```python
import torch

def _softmax(x, axis=-1):
    return torch.softmax(x, dim=axis)
def _gelu(x):
    return torch.nn.functional.gelu(x, approximate="tanh")
def _silu(x):
    return torch.nn.functional.silu(x)
def _sigmoid(x):
    return torch.sigmoid(x)
def _layernorm(x, axis=-1, eps=1e-5):
    m = x.mean(dim=axis, keepdim=True)
    v = x.var(dim=axis, keepdim=True, unbiased=False)
    return (x - m) / torch.sqrt(v + eps)
def _scatter_add(tgt, idx, src, axis=0):
    return tgt.clone().index_add_(axis, idx, src)
def _segment_sum(src, seg, n):
    out = torch.zeros((n,) + src.shape[1:], dtype=src.dtype)
    return out.index_add_(0, seg, src)
def _pad(x, pw):
    flat = tuple(v for pair in reversed(pw) for v in pair)
    return torch.nn.functional.pad(x, flat)

kernel_name = 'interleave_layers__sliding_block_cached__glu_gated_reglu__decode'

def setup_inputs():

    _w = (lambda a: (lambda t: t.to(torch.bfloat16) if t.is_floating_point() else t)(torch.as_tensor(a)))
    def _A_setup():
        B, Sq, D = 4, 1, 640
        Hq, Hkv, Sk, Dh = 32, 32, 4096, 128
        assert Hq % Hkv == 0, (Hq, Hkv)
        return {
            "x": _w((lambda *s: torch.randn(*s, dtype=torch.bfloat16) * 0.05)(B, Sq, D)),
            "k_cache": _w((lambda *s: torch.randn(*s, dtype=torch.bfloat16) * 0.8)(B, Hkv, Sk, Dh)),
            "v_cache": _w((lambda *s: torch.randn(*s, dtype=torch.bfloat16) * 0.8)(B, Hkv, Sk, Dh)),
            "wq": _w((lambda *s: torch.randn(*s, dtype=torch.bfloat16) * 0.8)(D, Hq * Dh) * (D ** -0.5)),
            "wo": _w((lambda *s: torch.randn(*s, dtype=torch.bfloat16) * 0.8)(Hq * Dh, D) * ((Hq * Dh) ** -0.5)),
        }
    _A_in = _A_setup()

    def _B_setup():
        B, S, D, F = 4, 1, 640, 1728
        return {
            "x": _w((lambda *s: torch.randn(*s, dtype=torch.bfloat16) * 0.05)(B, S, D)),
            "W_gate": _w((lambda *s: torch.randn(*s, dtype=torch.bfloat16) * 0.2)(D, F) * (2.0 / D) ** 0.5),
            "W_up": _w((lambda *s: torch.randn(*s, dtype=torch.bfloat16) * 0.2)(D, F) * (2.0 / D) ** 0.5),
            "W_down": _w((lambda *s: torch.randn(*s, dtype=torch.bfloat16) * 0.2)(F, D) * (1.0 / F) ** 0.5),
        }
    _B_in = _B_setup()

    return {
        "A_x": _A_in["x"],
        "A_k_cache": _A_in["k_cache"],
        "A_v_cache": _A_in["v_cache"],
        "A_wq": _A_in["wq"],
        "A_wo": _A_in["wo"],
        "B_x": _B_in["x"],
        "B_W_gate": _B_in["W_gate"],
        "B_W_up": _B_in["W_up"],
        "B_W_down": _B_in["W_down"],
    }

def reference(A_x, A_k_cache, A_v_cache, A_wq, A_wo, B_x, B_W_gate, B_W_up, B_W_down):
    def _A_ref(x, k_cache, v_cache, wq, wo):
        B, Sq, D = x.shape
        Hq, Dh = 32, 128
        Hkv, Sk = k_cache.shape[1], k_cache.shape[2]
        W = min(4096, max(256, Sk // 4))
        rep = Hq // Hkv
        q = (lambda x, *d: x.permute(*d))((x @ wq).reshape(B, Sq, Hq, Dh), 0, 2, 1, 3)
        k = (lambda x, n, axis: torch.repeat_interleave(x, n, dim=axis))(k_cache[:, :, Sk - W:, :], rep, axis=1)
        v = (lambda x, n, axis: torch.repeat_interleave(x, n, dim=axis))(v_cache[:, :, Sk - W:, :], rep, axis=1)
        scores = torch.einsum("bhqd,bhkd->bhqk", q, k) * (Dh ** -0.5)
        p = _softmax(scores, axis=-1)
        o = torch.einsum("bhqk,bhkd->bhqd", p, v)
        return (lambda x, *d: x.permute(*d))(o, 0, 2, 1, 3).reshape(B, Sq, Hq * Dh) @ wo

    def _B_ref(x, W_gate, W_up, W_down):
        gate = torch.einsum("bsd,df->bsf", x, W_gate)
        up = torch.einsum("bsd,df->bsf", x, W_up)
        gate = gate * (lambda x, dt: x.to(dt))(gate > 0, gate.dtype)
        h = gate * up
        return torch.einsum("bsf,fd->bsd", h, W_down)

    x = A_x
    x = x + _A_ref(_layernorm(x, axis=-1), A_k_cache, A_v_cache, A_wq, A_wo)
    x = x + _B_ref(_layernorm(x, axis=-1), B_W_gate, B_W_up, B_W_down)
    x = x + _A_ref(_layernorm(x, axis=-1), A_k_cache, A_v_cache, A_wq, A_wo)
    x = x + _B_ref(_layernorm(x, axis=-1), B_W_gate, B_W_up, B_W_down)
    return x
```

```cpp
#include <hip/hip_runtime.h>
#include <hip/hip_bf16.h>

typedef unsigned int u32;
typedef unsigned long long u64;
typedef unsigned short u16;
typedef u32 v4u __attribute__((ext_vector_type(4)));
typedef u32 v2u __attribute__((ext_vector_type(2)));
typedef float f4 __attribute__((ext_vector_type(4)));
typedef float f2 __attribute__((ext_vector_type(2)));
typedef __bf16 bf16x8 __attribute__((ext_vector_type(8)));
typedef short s16x4 __attribute__((ext_vector_type(4)));
#define LAS __attribute__((address_space(3)))

namespace {

constexpr int NWG = 256, NT = 512;
constexpr int DM = 640, SK = 4096, WIN = 1024, DH = 128, FF = 1728;

constexpr u32 WS_G = 0;
constexpr u32 WS_FLAG = 4096;
constexpr u32 WS_Q1 = 8192;
constexpr u32 WS_S = 262144;
constexpr u32 S_O0 = WS_S, S_OST0 = S_O0 + 65536, S_X1P0 = S_OST0 + 2048, S_HB0 = S_X1P0 + 40960, S_X2 = S_HB0 + 13824;
constexpr u32 S_O1 = S_X2 + 40960;
constexpr u32 PASS_STRIDE = S_O1 - S_O0;
constexpr u32 S_Q2 = S_O1 + (S_X2 - S_O0);
constexpr u32 WS_END = WS_S + 256 * 1536;
static_assert(S_Q2 + 65536 <= WS_END, "sentinel region too small");
static_assert((S_X2 % 128) == 0 && (S_O1 % 128) == 0 && (S_Q2 % 128) == 0 && (S_HB0 % 128) == 0, "alignment");
constexpr u32 WS_BYTES = 4u << 20;
constexpr u32 OOB = 0x80000000u;

constexpr u32 L_WR = 0;
constexpr u32 L_WQ = 110592;
constexpr u32 L_XN = 131072;
constexpr u32 XN_STRIDE = 1344;
constexpr u32 L_XR = L_XN + 4 * XN_STRIDE;
constexpr u32 L_RA = L_XR + 5120;
constexpr u32 L_RR = L_RA + 4096 + 128;
constexpr u32 L_RQ = L_RR + 9 * 512;
constexpr u32 L_QB = L_RQ + 5 * 512;
constexpr u32 L_END = L_QB + 2048;
static_assert(L_END <= 160 * 1024 && L_END > 80 * 1024, "LDS");

constexpr u32 SENT = 0xFFFFFFFFu;
constexpr u32 SPIN_MAX = 1u << 16;

#define SLP_S4 20
#define SLP_S5 50
#define SLP_X_NONE 90
#define SLP_X_S3 55
#define SLP_X_S4 20

struct Args {
  const u16* x; const u16* k; const u16* v; const u16* wq; const u16* wo;
  const u16* wg; const u16* wu; const u16* wd; u16* out; unsigned char* ws;
};

typedef __amdgpu_buffer_rsrc_t rsrc_t;
__device__ __forceinline__ rsrc_t mk_rsrc(const void* p, u32 bytes) { return __builtin_amdgcn_make_buffer_rsrc((void*)p, 0, (int)bytes, 0x00020000); }

__device__ __forceinline__ float bflo(u32 d) { return __builtin_bit_cast(float, d << 16); }
__device__ __forceinline__ float bfhi(u32 d) { return __builtin_bit_cast(float, d & 0xffff0000u); }
__device__ __forceinline__ u32 cvtpk(float lo, float hi) {
  typedef __bf16 bf16x2 __attribute__((ext_vector_type(2)));
  typedef float f32x2 __attribute__((ext_vector_type(2)));
  f32x2 v = {lo, hi};
  return __builtin_bit_cast(u32, __builtin_convertvector(v, bf16x2));
}
__device__ __forceinline__ u32 nosent(u32 d) { return d < 0xFFFFFFFEu ? d : 0xFFFFFFFEu; }
template <int CTRL> __device__ __forceinline__ float dpp(float x) {
  return __builtin_bit_cast(float, __builtin_amdgcn_mov_dpp(__builtin_bit_cast(int, x), CTRL, 0xf, 0xf, true));
}
template <int CTRL> __device__ __forceinline__ u32 dppu(u32 x) { return (u32)__builtin_amdgcn_mov_dpp((int)x, CTRL, 0xf, 0xf, true); }
template <int RM, int BM> __device__ __forceinline__ float sel(float old, float src) {
  return __builtin_bit_cast(float, __builtin_amdgcn_update_dpp(__builtin_bit_cast(int, old), __builtin_bit_cast(int, src), 0xE4, RM, BM, false));
}
constexpr int ROR8 = 0x128, ROR4 = 0x124, ROR2 = 0x122, ROR1 = 0x121, SHL1 = 0x101, SHL4 = 0x104, SHL8 = 0x108, SHL12 = 0x10C;
__device__ __forceinline__ float wave_sum(float x) {
  x += dpp<ROR8>(x); x += dpp<ROR4>(x); x += dpp<ROR2>(x); x += dpp<ROR1>(x);
  auto s = __builtin_amdgcn_permlane16_swap(__float_as_uint(x), __float_as_uint(x), false, false);
  x = __uint_as_float(s[0]) + __uint_as_float(s[1]);
  auto t = __builtin_amdgcn_permlane32_swap(__float_as_uint(x), __float_as_uint(x), false, false);
  return __uint_as_float(t[0]) + __uint_as_float(t[1]);
}
__device__ __forceinline__ float wave_max(float x) {
  x = fmaxf(x, dpp<ROR8>(x)); x = fmaxf(x, dpp<ROR4>(x)); x = fmaxf(x, dpp<ROR2>(x)); x = fmaxf(x, dpp<ROR1>(x));
  auto s = __builtin_amdgcn_permlane16_swap(__float_as_uint(x), __float_as_uint(x), false, false);
  x = fmaxf(__uint_as_float(s[0]), __uint_as_float(s[1]));
  auto t = __builtin_amdgcn_permlane32_swap(__float_as_uint(x), __float_as_uint(x), false, false);
  return fmaxf(__uint_as_float(t[0]), __uint_as_float(t[1]));
}
__device__ __forceinline__ f4 mfma(v4u a, v4u b, f4 c) {
  return __builtin_amdgcn_mfma_f32_16x16x32_bf16(__builtin_bit_cast(bf16x8, a), __builtin_bit_cast(bf16x8, b), c, 0, 0, 0);
}
__device__ __forceinline__ u32 umax3(u32 a, u32 b, u32 c) { u32 m = a > b ? a : b; return m > c ? m : c; }
__device__ __forceinline__ u32 umax4(v4u v) { return umax3(v.x, v.y, v.z > v.w ? v.z : v.w); }
__device__ __forceinline__ bool all_valid(u32 mx) { return __builtin_amdgcn_ballot_w64(mx == SENT) == 0ull; }
__device__ __forceinline__ void lds_barrier() {
  asm volatile("s_waitcnt lgkmcnt(0)" ::: "memory");
  __builtin_amdgcn_s_barrier();
  asm volatile("" ::: "memory");
}
__device__ __forceinline__ v4u wfrag(LAS unsigned char* p) {
  typedef short v4i16_t __attribute__((ext_vector_type(4)));
  v4i16_t lo = __builtin_amdgcn_ds_read_tr16_b64_v4i16((LAS v4i16_t*)p);
  v4i16_t hi = __builtin_amdgcn_ds_read_tr16_b64_v4i16((LAS v4i16_t*)(p + 256));
  v2u a = __builtin_bit_cast(v2u, lo), b = __builtin_bit_cast(v2u, hi);
  return v4u{a.x, a.y, b.x, b.y};
}
#define WAITVM(n) asm volatile("s_waitcnt vmcnt(" #n ")" ::: "memory")
__device__ __forceinline__ void dma16(u32 ldsaddr, u32 voff, rsrc_t rs, u32 soff) {
  asm volatile("s_mov_b32 m0, %0\n\ts_nop 0\n\tbuffer_load_dwordx4 %1, %2, %3 offen nt lds" ::"s"(ldsaddr), "v"(voff), "s"(rs), "s"(soff) : "memory");
}
__device__ __forceinline__ void st16_sc1(v4u d, u32 voff, rsrc_t rs) {
  asm volatile("buffer_store_dwordx4 %0, %1, %2, 0 offen sc1\n\ts_nop 1" ::"v"(d), "v"(voff), "s"(rs) : "memory");
}
__device__ __forceinline__ void st8_sc1(v2u d, u32 voff, rsrc_t rs) {
  asm volatile("buffer_store_dwordx2 %0, %1, %2, 0 offen sc1\n\ts_nop 1" ::"v"(d), "v"(voff), "s"(rs) : "memory");
}
__device__ __forceinline__ v4u quad_gather(u32 d) {
  return v4u{dppu<0x00>(d), dppu<0x55>(d), dppu<0xAA>(d), dppu<0xFF>(d)};
}
template <int N> __device__ __forceinline__ void idle() {
  if (N > 0) __builtin_amdgcn_s_sleep(N > 127 ? 127 : N);
}

#define LAUNDER_V(x) asm volatile("" : "+v"(x))
#define LAUNDER_S(x) asm volatile("" : "+s"(x))
typedef const __attribute__((address_space(4))) Args* kargs_t;
#define STAGE_VARS() int lane = lane_; LAUNDER_V(lane); const int g4 = lane >> 4, c16 = lane & 15; (void)g4; (void)c16; \
    int w = w_, c = c_; LAUNDER_S(w); LAUNDER_S(c); const int g = c & 7, r = c >> 3; (void)g; (void)r; \
    const int h = 4 * g + (r >> 3), bh = r & 7, b = bh >> 1, half = bh & 1; (void)h; (void)b; (void)half; \
    kargs_t ka = ka_; LAUNDER_S(ka); (void)ka

#define STAMP(i) do { } while (0)

__device__ __forceinline__ void ln_row(const float (&xa)[8], const float (&xb)[8], int lane, LAS unsigned char* dst) {
  const float sh = __builtin_bit_cast(float, __builtin_amdgcn_readfirstlane(__builtin_bit_cast(int, xa[0])));
  const float mb = lane < 16 ? 1.f : 0.f;
  float s1 = 0.f, s2 = 0.f, t1 = 0.f, t2 = 0.f;
#pragma unroll
  for (int j = 0; j < 8; ++j) {
    const float d = xa[j] - sh, e = xb[j] - sh;
    s1 += d; s2 = fmaf(d, d, s2); t1 += e; t2 = fmaf(e, e, t2);
  }
  s1 = wave_sum(fmaf(mb, t1, s1));
  s2 = wave_sum(fmaf(mb, t2, s2));
  const float md = s1 * (1.f / DM);
  const float var = fmaxf(fmaf(-md, md, s2 * (1.f / DM)), 0.f);
  const float rstd = __builtin_amdgcn_rsqf(var + 1e-5f);
  const float mean = sh + md;
  v4u oa, ob;
#pragma unroll
  for (int j = 0; j < 4; ++j) {
    oa[j] = cvtpk((xa[2 * j] - mean) * rstd, (xa[2 * j + 1] - mean) * rstd);
    ob[j] = cvtpk((xb[2 * j] - mean) * rstd, (xb[2 * j + 1] - mean) * rstd);
  }
  *(LAS v4u*)(dst + lane * 16) = oa;
  if (lane < 16) *(LAS v4u*)(dst + 1024 + lane * 16) = ob;
}

template <int NP, int STEP> __device__ __forceinline__ f2 red_sum(LAS unsigned char* base, int lane) {
  LAS unsigned char* p = base + ((lane >> 3) & 1) * 256 + (lane >> 4) * 64 + (lane & 7) * 8;
  f2 s = *(LAS f2*)p;
#pragma unroll
  for (int i = 1; i < NP; ++i) s += *(LAS f2*)(p + i * STEP * 512);
  return s;
}

__global__ void __launch_bounds__(NT, 2) fused_decode(Args a_) {
  extern __shared__ __attribute__((aligned(16))) unsigned char smem_[];
  LAS unsigned char* const L = (LAS unsigned char*)smem_;
  const int lane_ = threadIdx.x & 63;
  const int w_ = __builtin_amdgcn_readfirstlane(threadIdx.x >> 6);
  const int c_ = blockIdx.x;
  const kargs_t ka_ = (kargs_t)__builtin_amdgcn_kernarg_segment_ptr();
  const int role = ((c_ & 7) < 4 && (c_ >> 3) < 20) ? 3 : (((c_ & 7) == 4 || (c_ & 7) == 5) && (c_ >> 3) < 27) ? 4 : ((c_ & 7) == 6 && (c_ >> 3) < 20) ? 5 : 0;
  const rsrc_t rs_ws = mk_rsrc(a_.ws, WS_BYTES);
  u16* const outp = a_.out;

  v4u kr[16], vr[16];
  v4u xa, xb;
  u32 tag_lo, tag_hi;
#define KV_DESC() const size_t kvoff = ((size_t)(b * 32 + h) * SK + (SK - WIN) + half * 512 + w * 64) * DH; \
  const rsrc_t rs_k = mk_rsrc(ka->k + kvoff, 16384), rs_v = mk_rsrc(ka->v + kvoff, 16384); (void)rs_v; \
  const u32 voK0 = (lane & 7) * 1024 + ((lane >> 3) & 1) * 64 + g4 * 16, voK1 = voK0 + 8192, voV = lane * 16; (void)voV
#define LDK(i) asm volatile("buffer_load_dwordx4 %0, %1, %2, 0 offen offset:%3 nt" : "+a"(kr[i]) : "v"((((i) >> 1) & 1) ? voK1 : voK0), "s"(rs_k), "n"(((i) >> 2) * 256 + ((i) & 1) * 128) : "memory")
#define LDV(i) asm volatile("buffer_load_dwordx4 %0, %1, %2, %3 offen nt" : "+v"(vr[i]) : "v"(voV), "s"(rs_v), "s"((u32)((i) * 1024)) : "memory")
#define DMA_LANE(stride) ((u32)(lane >> 2) * (stride) + (u32)(((lane & 3) ^ (((lane >> 5) & 1) << 1)) * 16))
#define WF0() ((u32)((8 * g4 + (c16 >> 2)) * 64 + ((((c16 & 3) >> 1) ^ ((g4 & 1) << 1)) * 16) + (c16 & 1) * 8))

  {
    STAGE_VARS();
    KV_DESC();
    asm volatile("s_nop 4" ::: "memory");
    const v4u sv = {SENT, SENT, SENT, SENT};
    if (w < 2) st16_sc1(sv, (w * 64 + lane) < 96 ? WS_S + c * 1536 + (w * 64 + lane) * 16 : OOB, rs_ws);
    v2u gen;
    asm volatile("buffer_load_dwordx2 %0, %1, %2, 0 offen sc1" : "=v"(gen) : "v"(WS_G), "s"(rs_ws) : "memory");
    const rsrc_t rs_x = mk_rsrc(ka->x, 4 * DM * 2);
    const u32 o1 = w < 4 ? w * 1280 + lane * 16 : OOB, o2 = (w < 4 && lane < 16) ? w * 1280 + 1024 + lane * 16 : OOB;
    asm volatile("s_nop 4\n\tbuffer_load_dwordx4 %0, %2, %4, 0 offen\n\tbuffer_load_dwordx4 %1, %3, %4, 0 offen"
                 : "=&v"(xa), "=&v"(xb) : "v"(o1), "v"(o2), "s"(rs_x) : "memory");
    if (w >= 3) {
      const rsrc_t rs = mk_rsrc(ka->wq + (size_t)(320 * (bh & 1) + 64 * (w - 3)) * 4096 + h * 128 + 32 * (bh >> 1), 64 * 8192);
      const u32 vo = DMA_LANE(8192);
#pragma unroll
      for (int i = 0; i < 4; ++i) dma16(L_WQ + (w - 3) * 4096 + i * 1024, vo, rs, i * 16 * 8192);
    }
#pragma unroll
    for (int i = 0; i < 16; ++i) { asm volatile("" : "=a"(kr[i])); asm volatile("" : "=v"(vr[i])); }
    LDK(0); LDK(1); LDK(2); LDK(3); LDK(4); LDK(5);
    STAMP(0);
    WAITVM(6);
    asm volatile("" : "+v"(gen), "+v"(xa), "+v"(xb));
    const u64 tag64 = (((u64)(u32)__builtin_amdgcn_readfirstlane(gen.y) << 32) | (u32)__builtin_amdgcn_readfirstlane(gen.x)) + 1ull;
    tag_lo = (u32)tag64; tag_hi = (u32)(tag64 >> 32);
    lds_barrier();
    if (w == 7) st8_sc1(v2u{tag_lo, tag_hi}, lane == 0 ? WS_FLAG + c * 8 : OOB, rs_ws);
  }

#pragma unroll 1
  for (int it = 0; it < 2; ++it) {
    {
      STAGE_VARS();
      if (it == 1) {
        if (role == 0) idle<SLP_X_NONE>(); else if (role == 3) idle<SLP_X_S3>(); else if (role == 4) idle<SLP_X_S4>();
        if (w < 4) {
          const u32 base = S_X2 + g * 5120 + w * 64;
          const u32 o1 = base + (lane >> 2) * 256 + (lane & 3) * 16, o2 = lane < 16 ? base + (16 + (lane >> 2)) * 256 + (lane & 3) * 16 : OOB;
          _Pragma("nounroll") for (u32 spin = 0; spin < SPIN_MAX; ++spin) {
            asm volatile("buffer_load_dwordx4 %0, %2, %4, 0 offen sc1\n\tbuffer_load_dwordx4 %1, %3, %4, 0 offen sc1\n\ts_waitcnt vmcnt(0)"
                         : "=&v"(xa), "=&v"(xb) : "v"(o1), "v"(o2), "s"(rs_ws) : "memory");
            if (all_valid(umax3(umax4(xa), umax4(xb), 0u))) break;
          }
        }
      }
      STAMP(1 + it * 12);
      v4u wqf[4];
      if (w >= 3) {
        const u32 wf0 = WF0();
#pragma unroll
        for (int s = 0; s < 2; ++s)
#pragma unroll
          for (int t = 0; t < 2; ++t) wqf[s * 2 + t] = wfrag(L + L_WQ + (w - 3) * 4096 + s * 2048 + (wf0 ^ (t * 32)));
      }
      if (w < 4) {
        float fa[8], fb[8];
#pragma unroll
        for (int j = 0; j < 4; ++j) { fa[2 * j] = bflo(xa[j]); fa[2 * j + 1] = bfhi(xa[j]); fb[2 * j] = bflo(xb[j]); fb[2 * j + 1] = bfhi(xb[j]); }
        *(LAS v4u*)(L + L_XR + w * 1280 + lane * 16) = xa;
        if (lane < 16) *(LAS v4u*)(L + L_XR + w * 1280 + 1024 + lane * 16) = xb;
        ln_row(fa, fb, lane, L + L_XN + w * XN_STRIDE);
      }
      xa = v4u{0, 0, 0, 0}; xb = v4u{0, 0, 0, 0};
      lds_barrier();
      if (w >= 3) {
        f4 acc0 = {0, 0, 0, 0}, acc1 = {0, 0, 0, 0};
#pragma unroll
        for (int s = 0; s < 2; ++s) {
          const v4u xf = *(LAS v4u*)(L + L_XN + (c16 & 3) * XN_STRIDE + ((bh & 1) * 320 + (w - 3) * 64 + s * 32 + 8 * g4) * 2);
          acc0 = mfma(wqf[s * 2], xf, acc0);
          acc1 = mfma(wqf[s * 2 + 1], xf, acc1);
        }
        if (c16 < 4) {
          *(LAS f4*)(L + L_RQ + (w - 3) * 512 + c16 * 64 + g4 * 16) = acc0;
          *(LAS f4*)(L + L_RQ + (w - 3) * 512 + 256 + c16 * 64 + g4 * 16) = acc1;
        }
      }
      lds_barrier();
      if (w == 0) {
        const f2 s = red_sum<5, 1>(L + L_RQ, lane);
        const u32 d = nosent(cvtpk(s.x, s.y));
        if (it == 0) {
          const u32 dn = dppu<SHL1>(d);
          st16_sc1(v4u{d, dn, tag_lo, tag_hi}, (lane & 1) ? OOB : WS_Q1 + c * 512 + (lane >> 1) * 16, rs_ws);
        } else {
          st16_sc1(quad_gather(d), (lane & 3) ? OOB : S_Q2 + (h * 8 + (bh >> 1) * 2 + (bh & 1)) * 256 + lane * 4, rs_ws);
        }
      }
      STAMP(2 + it * 12);
    }
    v2u qv;
    if (it == 0) {
      STAGE_VARS();
      KV_DESC();
      asm volatile("s_nop 4" ::: "memory");
      const u32 q1off = WS_Q1 + (g + 8 * ((h & 3) * 8 + (lane >> 3))) * 512 + b * 128 + (lane & 7) * 16;
      v4u p1, fl0, fl1;
      WAITVM(3); LDK(6); WAITVM(3); LDK(7); WAITVM(3); LDK(8); WAITVM(3); LDK(9);
      WAITVM(3); LDK(10); WAITVM(3); LDK(11); WAITVM(3); LDK(12); WAITVM(3); LDK(13);
      WAITVM(3); LDK(14); WAITVM(3); LDK(15);
      WAITVM(3); LDV(0); WAITVM(3); LDV(1); WAITVM(3); LDV(2); WAITVM(3); LDV(3);
      asm volatile("buffer_load_dwordx4 %0, %1, %2, 0 offen sc1" : "=v"(p1) : "v"(q1off), "s"(rs_ws) : "memory");
      WAITVM(3); LDV(4); WAITVM(3); LDV(5); WAITVM(3); LDV(6); WAITVM(3); LDV(7);
      asm volatile("buffer_load_dwordx4 %0, %2, %3, 0 offen sc1\n\tbuffer_load_dwordx4 %1, %2, %3, 0 offen offset:1024 sc1"
                   : "=&v"(fl0), "=&v"(fl1) : "v"(w == 7 ? WS_FLAG + lane * 16 : OOB), "s"(rs_ws) : "memory");
      WAITVM(3); LDV(8); WAITVM(3); LDV(9); WAITVM(3); LDV(10); WAITVM(3); LDV(11);
      WAITVM(3); LDV(12); WAITVM(3); LDV(13); WAITVM(3); LDV(14); WAITVM(3); LDV(15);
      if (role == 3) {
        const rsrc_t rs = mk_rsrc(ka->wo + (size_t)(1024 * g + 128 * w) * DM + 32 * r, 128 * 1280);
        const u32 vo = DMA_LANE(1280);
        asm volatile("s_nop 4" ::: "memory");
#pragma unroll
        for (int i = 0; i < 8; ++i) dma16(L_WR + w * 8192 + i * 1024, vo, rs, i * 16 * 1280);
      }
      STAMP(3);
      asm volatile("" : "+v"(p1), "+v"(fl0), "+v"(fl1));
      _Pragma("nounroll") for (u32 spin = 0; spin < SPIN_MAX; ++spin) {
        if (__builtin_amdgcn_ballot_w64(p1.z != tag_lo || p1.w != tag_hi) == 0ull) break;
        asm volatile("buffer_load_dwordx4 %0, %1, %2, 0 offen sc1\n\ts_waitcnt vmcnt(0)" : "=&v"(p1) : "v"(q1off), "s"(rs_ws) : "memory");
      }
      qv = v2u{p1.x, p1.y};
      if (w == 7) {
        _Pragma("nounroll") for (u32 spin = 0; spin < SPIN_MAX; ++spin) {
          const bool bad = fl0.x != tag_lo || fl0.y != tag_hi || fl0.z != tag_lo || fl0.w != tag_hi || fl1.x != tag_lo || fl1.y != tag_hi || fl1.z != tag_lo || fl1.w != tag_hi;
          if (__builtin_amdgcn_ballot_w64(bad) == 0ull) break;
          asm volatile("buffer_load_dwordx4 %0, %2, %3, 0 offen sc1\n\tbuffer_load_dwordx4 %1, %2, %3, 0 offen offset:1024 sc1\n\ts_waitcnt vmcnt(0)"
                       : "=&v"(fl0), "=&v"(fl1) : "v"(WS_FLAG + lane * 16), "s"(rs_ws) : "memory");
        }
      }
    } else {
      STAGE_VARS();
      const u32 off = S_Q2 + ((h * 8 + (lane >> 3)) * 4 + b) * 64 + (lane & 7) * 8;
      _Pragma("nounroll") for (u32 spin = 0; spin < SPIN_MAX; ++spin) {
        asm volatile("buffer_load_dwordx2 %0, %1, %2, 0 offen sc1\n\ts_waitcnt vmcnt(0)" : "=&v"(qv) : "v"(off), "s"(rs_ws) : "memory");
        if (all_valid(qv.x > qv.y ? qv.x : qv.y)) break;
      }
    }
    STAMP(4 + it * 12);
    const u32 pbase = it * PASS_STRIDE;
    {
      STAGE_VARS();
      {
        float q0 = bflo(qv.x), q1 = bfhi(qv.x), q2 = bflo(qv.y), q3 = bfhi(qv.y);
        q0 += dpp<ROR8>(q0); q1 += dpp<ROR8>(q1); q2 += dpp<ROR8>(q2); q3 += dpp<ROR8>(q3);
        *(LAS v2u*)(L + L_QB + w * 256 + g4 * 64 + (lane & 7) * 8) = v2u{cvtpk(q0, q1), cvtpk(q2, q3)};
      }
      asm volatile("s_waitcnt lgkmcnt(0)" ::: "memory");
      const v4u qa0 = *(LAS v4u*)(L + L_QB + w * 256 + (c16 & 1) * 64 + g4 * 16);
      const v4u qa1 = *(LAS v4u*)(L + L_QB + w * 256 + 128 + (c16 & 1) * 64 + g4 * 16);
#pragma unroll
      for (int i = 0; i < 16; ++i) asm volatile("" : "+a"(kr[i]));
      f4 sc[8];
#pragma unroll
      for (int t = 0; t < 8; ++t) {
        sc[t] = mfma(qa0, kr[(t >> 1) * 4 + (t & 1) * 2], f4{0, 0, 0, 0});
        sc[t] = mfma(qa1, kr[(t >> 1) * 4 + (t & 1) * 2 + 1], sc[t]);
      }
      float sa = sc[0][0], sb = sc[1][0];
      sa = sel<0x1, 0xC>(sa, sc[1][1]); sb = sel<0x1, 0xC>(sb, sc[0][1]);
      sa = sel<0x2, 0x3>(sa, sc[2][0]); sb = sel<0x2, 0x3>(sb, sc[3][0]);
      sa = sel<0x2, 0xC>(sa, sc[3][1]); sb = sel<0x2, 0xC>(sb, sc[2][1]);
      sa = sel<0x4, 0x3>(sa, sc[4][0]); sb = sel<0x4, 0x3>(sb, sc[5][0]);
      sa = sel<0x4, 0xC>(sa, sc[5][1]); sb = sel<0x4, 0xC>(sb, sc[4][1]);
      sa = sel<0x8, 0x3>(sa, sc[6][0]); sb = sel<0x8, 0x3>(sb, sc[7][0]);
      sa = sel<0x8, 0xC>(sa, sc[7][1]); sb = sel<0x8, 0xC>(sb, sc[6][1]);
      const float s2 = (sa + dpp<ROR8>(sb)) * (0.08838834764831845f * 1.4426950408889634f);
      const float mw = wave_max(s2);
      const float pe = __builtin_amdgcn_exp2f(s2 - mw);
      const float lw = wave_sum(pe);
      const u32 pp = cvtpk(pe, dpp<SHL1>(pe));
      float oc[8] = {0, 0, 0, 0, 0, 0, 0, 0};
#define VPAIR(m) do { _Pragma("unroll") for (int u = 0; u < 4; ++u) { u32 t_; \
      asm volatile("v_perm_b32 %2, %1, %0, %3\n\tv_perm_b32 %1, %1, %0, %4\n\tv_mov_b32 %0, %2" : "+v"(vr[2 * (m)][u]), "+v"(vr[2 * (m) + 1][u]), "=&v"(t_) : "s"(0x05040100u), "s"(0x07060302u)); } } while (0)
#define PV(m, bc) asm volatile("s_nop 1\n\t" \
      "v_dot2c_f32_bf16_dpp %0, %8, %9 row_newbcast:" #bc " row_mask:0xf bank_mask:0xf\n\t" \
      "v_dot2c_f32_bf16_dpp %1, %8, %13 row_newbcast:" #bc " row_mask:0xf bank_mask:0xf\n\t" \
      "v_dot2c_f32_bf16_dpp %2, %8, %10 row_newbcast:" #bc " row_mask:0xf bank_mask:0xf\n\t" \
      "v_dot2c_f32_bf16_dpp %3, %8, %14 row_newbcast:" #bc " row_mask:0xf bank_mask:0xf\n\t" \
      "v_dot2c_f32_bf16_dpp %4, %8, %11 row_newbcast:" #bc " row_mask:0xf bank_mask:0xf\n\t" \
      "v_dot2c_f32_bf16_dpp %5, %8, %15 row_newbcast:" #bc " row_mask:0xf bank_mask:0xf\n\t" \
      "v_dot2c_f32_bf16_dpp %6, %8, %12 row_newbcast:" #bc " row_mask:0xf bank_mask:0xf\n\t" \
      "v_dot2c_f32_bf16_dpp %7, %8, %16 row_newbcast:" #bc " row_mask:0xf bank_mask:0xf\n\ts_nop 2" \
      : "+v"(oc[0]), "+v"(oc[1]), "+v"(oc[2]), "+v"(oc[3]), "+v"(oc[4]), "+v"(oc[5]), "+v"(oc[6]), "+v"(oc[7]) \
      : "v"(pp), "v"(vr[2 * (m)][0]), "v"(vr[2 * (m)][1]), "v"(vr[2 * (m)][2]), "v"(vr[2 * (m)][3]), \
        "v"(vr[2 * (m) + 1][0]), "v"(vr[2 * (m) + 1][1]), "v"(vr[2 * (m) + 1][2]), "v"(vr[2 * (m) + 1][3]))
      if (it == 0) {
#pragma unroll
        for (int i = 0; i < 12; ++i) asm volatile("" : "+v"(vr[i]));
        VPAIR(0); VPAIR(1); VPAIR(2); VPAIR(3); VPAIR(4); VPAIR(5);
      }
      PV(0, 0); PV(1, 2); PV(2, 4); PV(3, 6); PV(4, 8); PV(5, 10);
      if (it == 0) {
        if (role == 3) WAITVM(10); else WAITVM(2);
        asm volatile("" : "+v"(vr[12]), "+v"(vr[13]));
        VPAIR(6);
      }
      PV(6, 12);
      if (it == 0) {
        if (role == 3) WAITVM(8); else WAITVM(0);
        asm volatile("" : "+v"(vr[14]), "+v"(vr[15]));
        VPAIR(7);
      }
      PV(7, 14);
      STAMP(5 + it * 12);
      float t4[4], u2[2];
#pragma unroll
      for (int d = 0; d < 4; ++d) {
        auto s = __builtin_amdgcn_permlane32_swap(__float_as_uint(oc[d]), __float_as_uint(oc[d + 4]), false, false);
        t4[d] = __uint_as_float(s[0]) + __uint_as_float(s[1]);
      }
#pragma unroll
      for (int d = 0; d < 2; ++d) {
        auto s = __builtin_amdgcn_permlane16_swap(__float_as_uint(t4[d]), __float_as_uint(t4[d + 2]), false, false);
        u2[d] = __uint_as_float(s[0]) + __uint_as_float(s[1]);
      }
      *(LAS f2*)(L + L_RA + w * 512 + c16 * 32 + g4 * 8) = f2{u2[0], u2[1]};
      if (lane == 0) *(LAS f2*)(L + L_RA + 4096 + w * 8) = f2{mw, lw};
      lds_barrier();
      if (w == 0) {
        f2 st[8];
#pragma unroll
        for (int i = 0; i < 8; ++i) st[i] = *(LAS f2*)(L + L_RA + 4096 + i * 8);
        float M = st[0].x;
#pragma unroll
        for (int i = 1; i < 8; ++i) M = fmaxf(M, st[i].x);
        float Lsum = 0.f, o0 = 0.f, o1 = 0.f;
#pragma unroll
        for (int i = 0; i < 8; ++i) {
          const float f = __builtin_amdgcn_exp2f(st[i].x - M);
          const f2 v = *(LAS f2*)(L + L_RA + i * 512 + lane * 8);
          Lsum = fmaf(f, st[i].y, Lsum); o0 = fmaf(f, v.x, o0); o1 = fmaf(f, v.y, o1);
        }
        const float inv = __builtin_amdgcn_rcpf(Lsum);
        const u32 d = nosent(cvtpk(o0 * inv, o1 * inv));
        st16_sc1(quad_gather(d), (lane & 3) ? OOB : S_O0 + pbase + ((h * 2 + half) * 4 + b) * 256 + lane * 4, rs_ws);
        st8_sc1(v2u{nosent(__float_as_uint(M)), nosent(__float_as_uint(Lsum))}, lane == 0 ? S_OST0 + pbase + (h * 4 + b) * 16 + half * 8 : OOB, rs_ws);
      }
      STAMP(6 + it * 12);
    }
    if (role == 3) {
      STAGE_VARS();
      const int hh = 8 * g + w;
      const u32 o0off = S_O0 + pbase + (hh * 8 + (c16 & 3)) * 256 + (c16 >> 2) * 64 + g4 * 16;
      const u32 stoff = S_OST0 + pbase + (hh * 4 + (c16 & 3)) * 16;
      v4u oa, ob, st;
      asm volatile("buffer_load_dwordx4 %0, %3, %5, 0 offen sc1\n\tbuffer_load_dwordx4 %1, %3, %5, 0 offen offset:1024 sc1\n\tbuffer_load_dwordx4 %2, %4, %5, 0 offen sc1\n\ts_waitcnt vmcnt(3)"
                   : "=&v"(oa), "=&v"(ob), "=&v"(st) : "v"(o0off), "v"(stoff), "s"(rs_ws) : "memory");
      v4u wf[8];
      const u32 wf0 = WF0();
#pragma unroll
      for (int s = 0; s < 4; ++s)
#pragma unroll
        for (int t = 0; t < 2; ++t) wf[s * 2 + t] = wfrag(L + L_WR + w * 8192 + s * 2048 + (wf0 ^ (t * 32)));
      asm volatile("s_waitcnt vmcnt(0)" : "+v"(oa), "+v"(ob), "+v"(st)::"memory");
      _Pragma("nounroll") for (u32 spin = 0; spin < SPIN_MAX; ++spin) {
        if (all_valid(umax3(umax4(oa), umax4(ob), umax4(st)))) break;
        asm volatile("buffer_load_dwordx4 %0, %3, %5, 0 offen sc1\n\tbuffer_load_dwordx4 %1, %3, %5, 0 offen offset:1024 sc1\n\tbuffer_load_dwordx4 %2, %4, %5, 0 offen sc1\n\ts_waitcnt vmcnt(0)"
                     : "=&v"(oa), "=&v"(ob), "=&v"(st) : "v"(o0off), "v"(stoff), "s"(rs_ws) : "memory");
      }
      STAMP(7 + it * 12);
      const float m0 = __uint_as_float(st.x), l0 = __uint_as_float(st.y), m1 = __uint_as_float(st.z), l1 = __uint_as_float(st.w);
      const float mx = fmaxf(m0, m1);
      const float w0 = l0 * __builtin_amdgcn_exp2f(m0 - mx), w1 = l1 * __builtin_amdgcn_exp2f(m1 - mx);
      const float inv = __builtin_amdgcn_rcpf(w0 + w1), a0 = w0 * inv, a1 = w1 * inv;
      v4u of;
#pragma unroll
      for (int j = 0; j < 4; ++j) of[j] = cvtpk(fmaf(a0, bflo(oa[j]), a1 * bflo(ob[j])), fmaf(a0, bfhi(oa[j]), a1 * bfhi(ob[j])));
      f4 acc[8];
#pragma unroll
      for (int i = 0; i < 8; ++i) acc[i] = mfma(wf[i], of, f4{0, 0, 0, 0});
#pragma unroll
      for (int t = 0; t < 2; ++t) {
        f4 y;
#pragma unroll
        for (int j = 0; j < 4; ++j) y[j] = acc[t][j] + dpp<SHL4>(acc[2 + t][j]) + dpp<SHL8>(acc[4 + t][j]) + dpp<SHL12>(acc[6 + t][j]);
        if (c16 < 4) *(LAS f4*)(L + L_RR + w * 512 + t * 256 + c16 * 64 + g4 * 16) = y;
      }
      lds_barrier();
      if (w == 0) {
        f2 s = red_sum<8, 1>(L + L_RR, lane);
        if (g == 0) { const u32 x = *(LAS u32*)(L + L_XR + g4 * 1280 + (32 * r + 2 * c16) * 2); s.x += bflo(x); s.y += bfhi(x); }
        const u32 d = nosent(cvtpk(s.x, s.y));
        st16_sc1(quad_gather(d), (lane & 3) < 2 ? S_X1P0 + pbase + (lane & 3) * 20480 + (g * 20 + r) * 256 + (lane >> 2) * 16 : OOB, rs_ws);
      }
    } else if (role == 4) {
      STAGE_VARS();
      const int j4 = (g - 4) * 27 + r;
      if (it == 0) {
        lds_barrier();
        const rsrc_t rs = mk_rsrc(((w & 1) ? ka->wu : ka->wg) + (size_t)(160 * (w >> 1)) * FF + 32 * j4, 160 * 3456);
        const u32 vo = DMA_LANE(3456);
        asm volatile("s_nop 4" ::: "memory");
#pragma unroll
        for (int i = 0; i < 10; ++i) dma16(L_WR + w * 10240 + i * 1024, vo, rs, i * 16 * 3456);
        WAITVM(0);
      } else {
        idle<SLP_S4>();
      }
      v4u wf[10];
      const u32 wf0 = WF0();
#pragma unroll
      for (int s = 0; s < 5; ++s)
#pragma unroll
        for (int t = 0; t < 2; ++t) wf[s * 2 + t] = wfrag(L + L_WR + w * 10240 + s * 2048 + (wf0 ^ (t * 32)));
#pragma unroll
      for (int i = 0; i < 10; ++i) asm volatile("" : "+a"(wf[i]));
      if (w < 4) {
        const u32 base = S_X1P0 + pbase + (g - 4) * 20480 + w * 64;
        const u32 o1 = base + (lane >> 2) * 256 + (lane & 3) * 16, o2 = lane < 16 ? base + (16 + (lane >> 2)) * 256 + (lane & 3) * 16 : OOB;
        v4u pa[4], pb[4];
        _Pragma("nounroll") for (u32 spin = 0; spin < SPIN_MAX; ++spin) {
          asm volatile("buffer_load_dwordx4 %0, %8, %10, 0 offen sc1\n\tbuffer_load_dwordx4 %1, %9, %10, 0 offen sc1\n\t"
                       "buffer_load_dwordx4 %2, %8, %10, %11 offen sc1\n\tbuffer_load_dwordx4 %3, %9, %10, %11 offen sc1\n\t"
                       "buffer_load_dwordx4 %4, %8, %10, %12 offen sc1\n\tbuffer_load_dwordx4 %5, %9, %10, %12 offen sc1\n\t"
                       "buffer_load_dwordx4 %6, %8, %10, %13 offen sc1\n\tbuffer_load_dwordx4 %7, %9, %10, %13 offen sc1\n\ts_waitcnt vmcnt(0)"
                       : "=&v"(pa[0]), "=&v"(pb[0]), "=&v"(pa[1]), "=&v"(pb[1]), "=&v"(pa[2]), "=&v"(pb[2]), "=&v"(pa[3]), "=&v"(pb[3])
                       : "v"(o1), "v"(o2), "s"(rs_ws), "s"(5120u), "s"(10240u), "s"(15360u) : "memory");
          u32 mx = 0;
#pragma unroll
          for (int q = 0; q < 4; ++q) mx = umax3(mx, umax4(pa[q]), umax4(pb[q]));
          if (all_valid(mx)) break;
        }
        STAMP(8 + it * 12);
        float fa[8], fb[8];
#pragma unroll
        for (int j = 0; j < 4; ++j) {
          fa[2 * j] = (bflo(pa[0][j]) + bflo(pa[1][j])) + (bflo(pa[2][j]) + bflo(pa[3][j]));
          fa[2 * j + 1] = (bfhi(pa[0][j]) + bfhi(pa[1][j])) + (bfhi(pa[2][j]) + bfhi(pa[3][j]));
          fb[2 * j] = (bflo(pb[0][j]) + bflo(pb[1][j])) + (bflo(pb[2][j]) + bflo(pb[3][j]));
          fb[2 * j + 1] = (bfhi(pb[0][j]) + bfhi(pb[1][j])) + (bfhi(pb[2][j]) + bfhi(pb[3][j]));
        }
        ln_row(fa, fb, lane, L + L_XN + w * XN_STRIDE);
      }
      lds_barrier();
      f4 acc0 = {0, 0, 0, 0}, acc1 = {0, 0, 0, 0};
#pragma unroll
      for (int s = 0; s < 5; ++s) {
        const v4u xf = *(LAS v4u*)(L + L_XN + (c16 & 3) * XN_STRIDE + ((w >> 1) * 160 + s * 32 + 8 * g4) * 2);
        acc0 = mfma(wf[s * 2], xf, acc0);
        acc1 = mfma(wf[s * 2 + 1], xf, acc1);
      }
      if (c16 < 4) {
        *(LAS f4*)(L + L_RR + w * 512 + c16 * 64 + g4 * 16) = acc0;
        *(LAS f4*)(L + L_RR + w * 512 + 256 + c16 * 64 + g4 * 16) = acc1;
      }
      lds_barrier();
      if (w == 0) {
        const f2 ga = red_sum<4, 2>(L + L_RR, lane), up = red_sum<4, 2>(L + L_RR + 512, lane);
        const u32 d = nosent(cvtpk((ga.x > 0.f ? ga.x : 0.f * ga.x) * up.x, (ga.y > 0.f ? ga.y : 0.f * ga.y) * up.y));
        st16_sc1(quad_gather(d), (lane & 3) ? OOB : S_HB0 + pbase + j4 * 256 + lane * 4, rs_ws);
      }
    } else if (role == 5) {
      STAGE_VARS();
      const int s0 = w < 6 ? 7 * w : 42 + 6 * (w - 6);
      if (it == 0) {
        lds_barrier();
        const rsrc_t rs = mk_rsrc(ka->wd + (size_t)(32 * s0) * DM + 32 * r, (w < 6 ? 224 : 192) * 1280);
        const u32 vo = DMA_LANE(1280);
        asm volatile("s_nop 4" ::: "memory");
#pragma unroll
        for (int i = 0; i < 12; ++i) dma16(L_WR + s0 * 2048 + i * 1024, vo, rs, i * 16 * 1280);
        if (w < 6) { dma16(L_WR + s0 * 2048 + 12 * 1024, vo, rs, 12 * 16 * 1280); dma16(L_WR + s0 * 2048 + 13 * 1024, vo, rs, 13 * 16 * 1280); }
        WAITVM(0);
      }
      idle<SLP_S5>();
      v4u wf[14];
      const u32 wf0 = WF0();
#pragma unroll
      for (int s = 0; s < 6; ++s)
#pragma unroll
        for (int t = 0; t < 2; ++t) wf[s * 2 + t] = wfrag(L + L_WR + (s0 + s) * 2048 + (wf0 ^ (t * 32)));
      {
        const int s6 = w < 6 ? s0 + 6 : s0;
        wf[12] = wfrag(L + L_WR + s6 * 2048 + wf0); wf[13] = wfrag(L + L_WR + s6 * 2048 + (wf0 ^ 32));
      }
#pragma unroll
      for (int i = 0; i < 14; ++i) asm volatile("" : "+a"(wf[i]));
      const int slot = c16 >> 2;
      const u32 hoff = S_HB0 + pbase + (c16 & 3) * 64 + g4 * 16;
      const u32 oA = hoff + (s0 + slot) * 256;
      const u32 oB = (slot < (w < 6 ? 3 : 2)) ? hoff + (s0 + 4 + slot) * 256 : OOB;
      v4u ha, hb;
      u32 rx0, rx1, rx2, rx3;
      const u32 rxo = w == 7 ? S_X1P0 + pbase + r * 256 + lane * 4 : OOB;
      _Pragma("nounroll") for (u32 spin = 0; spin < SPIN_MAX; ++spin) {
        asm volatile("buffer_load_dwordx4 %0, %6, %9, 0 offen sc1\n\tbuffer_load_dwordx4 %1, %7, %9, 0 offen sc1\n\t"
                     "buffer_load_dword %2, %8, %9, 0 offen sc1\n\tbuffer_load_dword %3, %8, %9, %10 offen sc1\n\tbuffer_load_dword %4, %8, %9, %11 offen sc1\n\tbuffer_load_dword %5, %8, %9, %12 offen sc1\n\ts_waitcnt vmcnt(0)"
                     : "=&v"(ha), "=&v"(hb), "=&v"(rx0), "=&v"(rx1), "=&v"(rx2), "=&v"(rx3)
                     : "v"(oA), "v"(oB), "v"(rxo), "s"(rs_ws), "s"(5120u), "s"(10240u), "s"(15360u) : "memory");
        if (all_valid(umax3(umax3(umax4(ha), umax4(hb), rx0), umax3(rx1, rx2, rx3), 0u))) break;
      }
      STAMP(9 + it * 12);
      f4 acc[8];
#pragma unroll
      for (int i = 0; i < 8; ++i) acc[i] = mfma(wf[i], ha, f4{0, 0, 0, 0});
#pragma unroll
      for (int i = 0; i < 4; ++i) acc[i] = mfma(wf[8 + i], hb, acc[i]);
      if (w < 6) { acc[4] = mfma(wf[12], hb, acc[4]); acc[5] = mfma(wf[13], hb, acc[5]); }
#pragma unroll
      for (int t = 0; t < 2; ++t) {
        f4 y;
#pragma unroll
        for (int j = 0; j < 4; ++j) y[j] = acc[t][j] + dpp<SHL4>(acc[2 + t][j]) + dpp<SHL8>(acc[4 + t][j]) + dpp<SHL12>(acc[6 + t][j]);
        if (c16 < 4) *(LAS f4*)(L + L_RR + w * 512 + t * 256 + c16 * 64 + g4 * 16) = y;
      }
      if (w == 7)
        *(LAS f2*)(L + L_RR + 8 * 512 + ((lane >> 3) & 1) * 256 + g4 * 64 + (lane & 7) * 8) =
            f2{(bflo(rx0) + bflo(rx1)) + (bflo(rx2) + bflo(rx3)), (bfhi(rx0) + bfhi(rx1)) + (bfhi(rx2) + bfhi(rx3))};
      lds_barrier();
      if (w == 0) {
        const f2 s = red_sum<9, 1>(L + L_RR, lane);
        const u32 d0 = cvtpk(s.x, s.y);
        if (it == 0) {
          const v4u q = quad_gather(nosent(d0));
          const u32 off = S_X2 + (lane & 3) * 5120 + r * 256 + (lane >> 2) * 16;
          st16_sc1(q, off, rs_ws);
          st16_sc1(q, off + 4 * 5120, rs_ws);
        } else {
          const v4u q = quad_gather(d0);
          const rsrc_t rs_o = mk_rsrc(outp, 4 * DM * 2);
          asm volatile("s_nop 4\n\tbuffer_store_dwordx4 %0, %1, %2, 0 offen\n\ts_nop 1" ::"v"(q), "v"((lane & 3) ? OOB : (u32)(g4 * 1280 + r * 64 + (c16 >> 2) * 16)), "s"(rs_o) : "memory");
          if (r == 0) st8_sc1(v2u{tag_lo, tag_hi}, lane == 0 ? WS_G : OOB, rs_ws);
        }
      }
    }
    STAMP(10 + it * 12);
  }
}

}

extern "C" void kernel_launch(void* const* d_in, const int* in_sizes, int n_in, void* d_out, int out_size, void* d_ws,
                              size_t ws_size, hipStream_t stream) {
  Args a;
  a.x = (const u16*)d_in[0];
  a.k = (const u16*)d_in[1];
  a.v = (const u16*)d_in[2];
  a.wq = (const u16*)d_in[3];
  a.wo = (const u16*)d_in[4];
  a.wg = (const u16*)d_in[6];
  a.wu = (const u16*)d_in[7];
  a.wd = (const u16*)d_in[8];
  a.out = (u16*)d_out;
  a.ws = (unsigned char*)d_ws;
  (void)hipFuncSetAttribute((const void*)fused_decode, hipFuncAttributeMaxDynamicSharedMemorySize, (int)L_END);
  hipLaunchKernelGGL(fused_decode, dim3(NWG), dim3(NT), L_END, stream, a);
}
```

```cpp
#include <hip/hip_runtime.h>
#include <hip/hip_bf16.h>

typedef unsigned int u32;
typedef unsigned long long u64;
typedef unsigned short u16;
typedef u32 v4u __attribute__((ext_vector_type(4)));
typedef u32 v2u __attribute__((ext_vector_type(2)));
typedef float f4 __attribute__((ext_vector_type(4)));
typedef float f2 __attribute__((ext_vector_type(2)));
typedef __bf16 bf16x8 __attribute__((ext_vector_type(8)));
typedef short s16x4 __attribute__((ext_vector_type(4)));
#define LAS __attribute__((address_space(3)))

namespace {

constexpr int NWG = 256, NT = 512;
constexpr int DM = 640, SK = 4096, WIN = 1024, DH = 128, FF = 1728;

constexpr u32 WS_G = 0;
constexpr u32 WS_FLAG = 4096;
constexpr u32 WS_Q1 = 8192;
constexpr u32 WS_S = 262144;
constexpr u32 S_O0 = WS_S, S_OST0 = S_O0 + 65536, S_X1P0 = S_OST0 + 2048, S_HB0 = S_X1P0 + 40960, S_X2 = S_HB0 + 13824;
constexpr u32 S_O1 = S_X2 + 40960;
constexpr u32 PASS_STRIDE = S_O1 - S_O0;
constexpr u32 S_Q2 = S_O1 + (S_X2 - S_O0);
constexpr u32 WS_END = WS_S + 256 * 1536;
static_assert(S_Q2 + 65536 <= WS_END, "sentinel region too small");
static_assert((S_X2 % 128) == 0 && (S_O1 % 128) == 0 && (S_Q2 % 128) == 0 && (S_HB0 % 128) == 0, "alignment");
constexpr u32 WS_BYTES = 4u << 20;
constexpr u32 OOB = 0x80000000u;

constexpr u32 L_WR = 0;
constexpr u32 L_WQ = 110592;
constexpr u32 L_XN = 131072;
constexpr u32 XN_STRIDE = 1344;
constexpr u32 L_XR = L_XN + 4 * XN_STRIDE;
constexpr u32 L_RA = L_XR + 5120;
constexpr u32 L_RR = L_RA + 4096 + 128;
constexpr u32 L_RQ = L_RR + 9 * 512;
constexpr u32 L_QB = L_RQ + 5 * 512;
constexpr u32 L_END = L_QB + 2048;
static_assert(L_END <= 160 * 1024 && L_END > 80 * 1024, "LDS");

constexpr u32 SENT = 0xFFFFFFFFu;
constexpr u32 SPIN_MAX = 1u << 16;

#define SLP_S4 20
#define SLP_S5 50
#define SLP_X_NONE 90
#define SLP_X_S3 55
#define SLP_X_S4 20

struct Args {
  const u16* x; const u16* k; const u16* v; const u16* wq; const u16* wo;
  const u16* wg; const u16* wu; const u16* wd; u16* out; unsigned char* ws;
};

typedef __amdgpu_buffer_rsrc_t rsrc_t;
__device__ __forceinline__ rsrc_t mk_rsrc(const void* p, u32 bytes) { return __builtin_amdgcn_make_buffer_rsrc((void*)p, 0, (int)bytes, 0x00020000); }

__device__ __forceinline__ float bflo(u32 d) { return __builtin_bit_cast(float, d << 16); }
__device__ __forceinline__ float bfhi(u32 d) { return __builtin_bit_cast(float, d & 0xffff0000u); }
__device__ __forceinline__ u32 cvtpk(float lo, float hi) {
  typedef __bf16 bf16x2 __attribute__((ext_vector_type(2)));
  typedef float f32x2 __attribute__((ext_vector_type(2)));
  f32x2 v = {lo, hi};
  return __builtin_bit_cast(u32, __builtin_convertvector(v, bf16x2));
}
__device__ __forceinline__ u32 nosent(u32 d) { return d < 0xFFFFFFFEu ? d : 0xFFFFFFFEu; }
template <int CTRL> __device__ __forceinline__ float dpp(float x) {
  return __builtin_bit_cast(float, __builtin_amdgcn_mov_dpp(__builtin_bit_cast(int, x), CTRL, 0xf, 0xf, true));
}
template <int CTRL> __device__ __forceinline__ u32 dppu(u32 x) { return (u32)__builtin_amdgcn_mov_dpp((int)x, CTRL, 0xf, 0xf, true); }
template <int RM, int BM> __device__ __forceinline__ float sel(float old, float src) {
  return __builtin_bit_cast(float, __builtin_amdgcn_update_dpp(__builtin_bit_cast(int, old), __builtin_bit_cast(int, src), 0xE4, RM, BM, false));
}
constexpr int ROR8 = 0x128, ROR4 = 0x124, ROR2 = 0x122, ROR1 = 0x121, SHL1 = 0x101, SHL4 = 0x104, SHL8 = 0x108, SHL12 = 0x10C;
__device__ __forceinline__ float wave_sum(float x) {
  x += dpp<ROR8>(x); x += dpp<ROR4>(x); x += dpp<ROR2>(x); x += dpp<ROR1>(x);
  auto s = __builtin_amdgcn_permlane16_swap(__float_as_uint(x), __float_as_uint(x), false, false);
  x = __uint_as_float(s[0]) + __uint_as_float(s[1]);
  auto t = __builtin_amdgcn_permlane32_swap(__float_as_uint(x), __float_as_uint(x), false, false);
  return __uint_as_float(t[0]) + __uint_as_float(t[1]);
}
__device__ __forceinline__ float wave_max(float x) {
  x = fmaxf(x, dpp<ROR8>(x)); x = fmaxf(x, dpp<ROR4>(x)); x = fmaxf(x, dpp<ROR2>(x)); x = fmaxf(x, dpp<ROR1>(x));
  auto s = __builtin_amdgcn_permlane16_swap(__float_as_uint(x), __float_as_uint(x), false, false);
  x = fmaxf(__uint_as_float(s[0]), __uint_as_float(s[1]));
  auto t = __builtin_amdgcn_permlane32_swap(__float_as_uint(x), __float_as_uint(x), false, false);
  return fmaxf(__uint_as_float(t[0]), __uint_as_float(t[1]));
}
__device__ __forceinline__ f4 mfma(v4u a, v4u b, f4 c) {
  return __builtin_amdgcn_mfma_f32_16x16x32_bf16(__builtin_bit_cast(bf16x8, a), __builtin_bit_cast(bf16x8, b), c, 0, 0, 0);
}
__device__ __forceinline__ u32 umax3(u32 a, u32 b, u32 c) { u32 m = a > b ? a : b; return m > c ? m : c; }
__device__ __forceinline__ u32 umax4(v4u v) { return umax3(v.x, v.y, v.z > v.w ? v.z : v.w); }
__device__ __forceinline__ bool all_valid(u32 mx) { return __builtin_amdgcn_ballot_w64(mx == SENT) == 0ull; }
__device__ __forceinline__ void lds_barrier() {
  asm volatile("s_waitcnt lgkmcnt(0)" ::: "memory");
  __builtin_amdgcn_s_barrier();
  asm volatile("" ::: "memory");
}
__device__ __forceinline__ v4u wfrag(LAS unsigned char* p) {
  typedef short v4i16_t __attribute__((ext_vector_type(4)));
  v4i16_t lo = __builtin_amdgcn_ds_read_tr16_b64_v4i16((LAS v4i16_t*)p);
  v4i16_t hi = __builtin_amdgcn_ds_read_tr16_b64_v4i16((LAS v4i16_t*)(p + 256));
  v2u a = __builtin_bit_cast(v2u, lo), b = __builtin_bit_cast(v2u, hi);
  return v4u{a.x, a.y, b.x, b.y};
}
#define WAITVM(n) asm volatile("s_waitcnt vmcnt(" #n ")" ::: "memory")
__device__ __forceinline__ void dma16(u32 ldsaddr, u32 voff, rsrc_t rs, u32 soff) {
  asm volatile("s_mov_b32 m0, %0\n\ts_nop 0\n\tbuffer_load_dwordx4 %1, %2, %3 offen nt lds" ::"s"(ldsaddr), "v"(voff), "s"(rs), "s"(soff) : "memory");
}
__device__ __forceinline__ void st16_sc1(v4u d, u32 voff, rsrc_t rs) {
  asm volatile("buffer_store_dwordx4 %0, %1, %2, 0 offen sc1\n\ts_nop 1" ::"v"(d), "v"(voff), "s"(rs) : "memory");
}
__device__ __forceinline__ void st8_sc1(v2u d, u32 voff, rsrc_t rs) {
  asm volatile("buffer_store_dwordx2 %0, %1, %2, 0 offen sc1\n\ts_nop 1" ::"v"(d), "v"(voff), "s"(rs) : "memory");
}
__device__ __forceinline__ v4u quad_gather(u32 d) {
  return v4u{dppu<0x00>(d), dppu<0x55>(d), dppu<0xAA>(d), dppu<0xFF>(d)};
}
template <int N> __device__ __forceinline__ void idle() {
  if (N > 0) __builtin_amdgcn_s_sleep(N > 127 ? 127 : N);
}

#define LAUNDER_V(x) asm volatile("" : "+v"(x))
#define LAUNDER_S(x) asm volatile("" : "+s"(x))
typedef const __attribute__((address_space(4))) Args* kargs_t;
#define STAGE_VARS() int lane = lane_; LAUNDER_V(lane); const int g4 = lane >> 4, c16 = lane & 15; (void)g4; (void)c16; \
    int w = w_, c = c_; LAUNDER_S(w); LAUNDER_S(c); const int g = c & 7, r = c >> 3; (void)g; (void)r; \
    const int h = 4 * g + (r >> 3), bh = r & 7, b = bh >> 1, half = bh & 1; (void)h; (void)b; (void)half; \
    kargs_t ka = ka_; LAUNDER_S(ka); (void)ka

#define STAMP(i) do { } while (0)
#define STAMP_OUT() do { } while (0)

__device__ __forceinline__ void ln_row(const float (&xa)[8], const float (&xb)[8], int lane, LAS unsigned char* dst) {
  const float sh = __builtin_bit_cast(float, __builtin_amdgcn_readfirstlane(__builtin_bit_cast(int, xa[0])));
  const float mb = lane < 16 ? 1.f : 0.f;
  float s1 = 0.f, s2 = 0.f, t1 = 0.f, t2 = 0.f;
#pragma unroll
  for (int j = 0; j < 8; ++j) {
    const float d = xa[j] - sh, e = xb[j] - sh;
    s1 += d; s2 = fmaf(d, d, s2); t1 += e; t2 = fmaf(e, e, t2);
  }
  s1 = wave_sum(fmaf(mb, t1, s1));
  s2 = wave_sum(fmaf(mb, t2, s2));
  const float md = s1 * (1.f / DM);
  const float var = fmaxf(fmaf(-md, md, s2 * (1.f / DM)), 0.f);
  const float rstd = __builtin_amdgcn_rsqf(var + 1e-5f);
  const float mean = sh + md;
  v4u oa, ob;
#pragma unroll
  for (int j = 0; j < 4; ++j) {
    oa[j] = cvtpk((xa[2 * j] - mean) * rstd, (xa[2 * j + 1] - mean) * rstd);
    ob[j] = cvtpk((xb[2 * j] - mean) * rstd, (xb[2 * j + 1] - mean) * rstd);
  }
  *(LAS v4u*)(dst + lane * 16) = oa;
  if (lane < 16) *(LAS v4u*)(dst + 1024 + lane * 16) = ob;
}

template <int NP, int STEP> __device__ __forceinline__ f2 red_sum(LAS unsigned char* base, int lane) {
  LAS unsigned char* p = base + ((lane >> 3) & 1) * 256 + (lane >> 4) * 64 + (lane & 7) * 8;
  f2 s = *(LAS f2*)p;
#pragma unroll
  for (int i = 1; i < NP; ++i) s += *(LAS f2*)(p + i * STEP * 512);
  return s;
}

__global__ void __launch_bounds__(NT, 2) fused_decode(Args a_) {
  extern __shared__ __attribute__((aligned(16))) unsigned char smem_[];
  LAS unsigned char* const L = (LAS unsigned char*)smem_;
  const int lane_ = threadIdx.x & 63;
  const int w_ = __builtin_amdgcn_readfirstlane(threadIdx.x >> 6);
  const int c_ = blockIdx.x;
  const kargs_t ka_ = (kargs_t)__builtin_amdgcn_kernarg_segment_ptr();
  const int role = ((c_ & 7) < 4 && (c_ >> 3) < 20) ? 3 : (((c_ & 7) == 4 || (c_ & 7) == 5) && (c_ >> 3) < 27) ? 4 : ((c_ & 7) == 6 && (c_ >> 3) < 20) ? 5 : 0;
  const rsrc_t rs_ws = mk_rsrc(a_.ws, WS_BYTES);
  u16* const outp = a_.out;

  STAMP(11);
  v4u kr[16], vr[16];
  v4u xa, xb;
  u32 tag_lo, tag_hi;
#define KV_DESC() const size_t kvoff = ((size_t)(b * 32 + h) * SK + (SK - WIN) + half * 512 + w * 64) * DH; \
  const rsrc_t rs_k = mk_rsrc(ka->k + kvoff, 16384), rs_v = mk_rsrc(ka->v + kvoff, 16384); (void)rs_v; \
  const u32 voK0 = (lane & 7) * 1024 + ((lane >> 3) & 1) * 64 + g4 * 16, voK1 = voK0 + 8192, voV = lane * 16; (void)voV
#define LDK(i) asm volatile("buffer_load_dwordx4 %0, %1, %2, 0 offen offset:%3 nt" : "+a"(kr[i]) : "v"((((i) >> 1) & 1) ? voK1 : voK0), "s"(rs_k), "n"(((i) >> 2) * 256 + ((i) & 1) * 128) : "memory")
#define LDV(i) asm volatile("buffer_load_dwordx4 %0, %1, %2, %3 offen nt" : "+v"(vr[i]) : "v"(voV), "s"(rs_v), "s"((u32)((i) * 1024)) : "memory")
#define DMA_LANE(stride) ((u32)(lane >> 2) * (stride) + (u32)(((lane & 3) ^ (((lane >> 5) & 1) << 1)) * 16))
#define WF0() ((u32)((8 * g4 + (c16 >> 2)) * 64 + ((((c16 & 3) >> 1) ^ ((g4 & 1) << 1)) * 16) + (c16 & 1) * 8))

  {
    STAGE_VARS();
    KV_DESC();
    asm volatile("s_nop 4" ::: "memory");
    v4u junk = {0, 0, 0, 0};
    {
      u64 pc; u32 len;
      asm volatile("s_getpc_b64 %0\n.Lfd_code_anchor:\n\ts_mov_b32 %1, .Lfd_code_end-.Lfd_code_anchor" : "=s"(pc), "=s"(len));
      const rsrc_t rs_c = mk_rsrc((const void*)pc, len);
      asm volatile("s_nop 4\n\tbuffer_load_dwordx4 %0, %1, %2, 0 offen" : "=v"(junk) : "v"(w == 7 ? (u32)(r * 1024 + lane * 16) : OOB), "s"(rs_c) : "memory");
    }
    const v4u sv = {SENT, SENT, SENT, SENT};
    if (w < 2) st16_sc1(sv, (w * 64 + lane) < 96 ? WS_S + c * 1536 + (w * 64 + lane) * 16 : OOB, rs_ws);
    v2u gen;
    asm volatile("buffer_load_dwordx2 %0, %1, %2, 0 offen sc1" : "=v"(gen) : "v"(WS_G), "s"(rs_ws) : "memory");
    const rsrc_t rs_x = mk_rsrc(ka->x, 4 * DM * 2);
    const u32 o1 = w < 4 ? w * 1280 + lane * 16 : OOB, o2 = (w < 4 && lane < 16) ? w * 1280 + 1024 + lane * 16 : OOB;
    asm volatile("s_nop 4\n\tbuffer_load_dwordx4 %0, %2, %4, 0 offen\n\tbuffer_load_dwordx4 %1, %3, %4, 0 offen"
                 : "=&v"(xa), "=&v"(xb) : "v"(o1), "v"(o2), "s"(rs_x) : "memory");
    if (w >= 3) {
      const rsrc_t rs = mk_rsrc(ka->wq + (size_t)(320 * (bh & 1) + 64 * (w - 3)) * 4096 + h * 128 + 32 * (bh >> 1), 64 * 8192);
      const u32 vo = DMA_LANE(8192);
#pragma unroll
      for (int i = 0; i < 4; ++i) dma16(L_WQ + (w - 3) * 4096 + i * 1024, vo, rs, i * 16 * 8192);
    }
#pragma unroll
    for (int i = 0; i < 16; ++i) { asm volatile("" : "=a"(kr[i])); asm volatile("" : "=v"(vr[i])); }
    LDK(0); LDK(1); LDK(2); LDK(3); LDK(4); LDK(5);
    STAMP(0);
    WAITVM(6);
    asm volatile("" : "+v"(gen), "+v"(xa), "+v"(xb), "+v"(junk));
    const u64 tag64 = (((u64)(u32)__builtin_amdgcn_readfirstlane(gen.y) << 32) | (u32)__builtin_amdgcn_readfirstlane(gen.x)) + 1ull;
    tag_lo = (u32)tag64; tag_hi = (u32)(tag64 >> 32);
    lds_barrier();
    if (w == 7) st8_sc1(v2u{tag_lo, tag_hi}, lane == 0 ? WS_FLAG + c * 8 : OOB, rs_ws);
  }

#pragma unroll 1
  for (int it = 0; it < 2; ++it) {
    {
      STAGE_VARS();
      if (it == 1) {
        if (role == 0) idle<SLP_X_NONE>(); else if (role == 3) idle<SLP_X_S3>(); else if (role == 4) idle<SLP_X_S4>();
        if (w < 4) {
          const u32 base = S_X2 + g * 5120 + w * 64;
          const u32 o1 = base + (lane >> 2) * 256 + (lane & 3) * 16, o2 = lane < 16 ? base + (16 + (lane >> 2)) * 256 + (lane & 3) * 16 : OOB;
          _Pragma("nounroll") for (u32 spin = 0; spin < SPIN_MAX; ++spin) {
            asm volatile("buffer_load_dwordx4 %0, %2, %4, 0 offen sc1\n\tbuffer_load_dwordx4 %1, %3, %4, 0 offen sc1\n\ts_waitcnt vmcnt(0)"
                         : "=&v"(xa), "=&v"(xb) : "v"(o1), "v"(o2), "s"(rs_ws) : "memory");
            if (all_valid(umax3(umax4(xa), umax4(xb), 0u))) break;
          }
        }
      }
      STAMP(1 + it * 12);
      v4u wqf[4];
      if (w >= 3) {
        const u32 wf0 = WF0();
#pragma unroll
        for (int s = 0; s < 2; ++s)
#pragma unroll
          for (int t = 0; t < 2; ++t) wqf[s * 2 + t] = wfrag(L + L_WQ + (w - 3) * 4096 + s * 2048 + (wf0 ^ (t * 32)));
      }
      if (w < 4) {
        float fa[8], fb[8];
#pragma unroll
        for (int j = 0; j < 4; ++j) { fa[2 * j] = bflo(xa[j]); fa[2 * j + 1] = bfhi(xa[j]); fb[2 * j] = bflo(xb[j]); fb[2 * j + 1] = bfhi(xb[j]); }
        *(LAS v4u*)(L + L_XR + w * 1280 + lane * 16) = xa;
        if (lane < 16) *(LAS v4u*)(L + L_XR + w * 1280 + 1024 + lane * 16) = xb;
        ln_row(fa, fb, lane, L + L_XN + w * XN_STRIDE);
      }
      xa = v4u{0, 0, 0, 0}; xb = v4u{0, 0, 0, 0};
      lds_barrier();
      if (w >= 3) {
        f4 acc0 = {0, 0, 0, 0}, acc1 = {0, 0, 0, 0};
#pragma unroll
        for (int s = 0; s < 2; ++s) {
          const v4u xf = *(LAS v4u*)(L + L_XN + (c16 & 3) * XN_STRIDE + ((bh & 1) * 320 + (w - 3) * 64 + s * 32 + 8 * g4) * 2);
          acc0 = mfma(wqf[s * 2], xf, acc0);
          acc1 = mfma(wqf[s * 2 + 1], xf, acc1);
        }
        if (c16 < 4) {
          *(LAS f4*)(L + L_RQ + (w - 3) * 512 + c16 * 64 + g4 * 16) = acc0;
          *(LAS f4*)(L + L_RQ + (w - 3) * 512 + 256 + c16 * 64 + g4 * 16) = acc1;
        }
      }
      lds_barrier();
      if (w == 0) {
        const f2 s = red_sum<5, 1>(L + L_RQ, lane);
        const u32 d = nosent(cvtpk(s.x, s.y));
        if (it == 0) {
          const u32 dn = dppu<SHL1>(d);
          st16_sc1(v4u{d, dn, tag_lo, tag_hi}, (lane & 1) ? OOB : WS_Q1 + c * 512 + (lane >> 1) * 16, rs_ws);
        } else {
          st16_sc1(quad_gather(d), (lane & 3) ? OOB : S_Q2 + (h * 8 + (bh >> 1) * 2 + (bh & 1)) * 256 + lane * 4, rs_ws);
        }
      }
      STAMP(2 + it * 12);
    }
    v2u qv;
    if (it == 0) {
      STAGE_VARS();
      KV_DESC();
      asm volatile("s_nop 4" ::: "memory");
      const u32 q1off = WS_Q1 + (g + 8 * ((h & 3) * 8 + (lane >> 3))) * 512 + b * 128 + (lane & 7) * 16;
      v4u p1, fl0, fl1;
      WAITVM(3); LDK(6); WAITVM(3); LDK(7); WAITVM(3); LDK(8); WAITVM(3); LDK(9);
      WAITVM(3); LDK(10); WAITVM(3); LDK(11); WAITVM(3); LDK(12); WAITVM(3); LDK(13);
      WAITVM(3); LDK(14); WAITVM(3); LDK(15);
      WAITVM(3); LDV(0); WAITVM(3); LDV(1); WAITVM(3); LDV(2); WAITVM(3); LDV(3);
      asm volatile("buffer_load_dwordx4 %0, %1, %2, 0 offen sc1" : "=v"(p1) : "v"(q1off), "s"(rs_ws) : "memory");
      WAITVM(3); LDV(4); WAITVM(3); LDV(5); WAITVM(3); LDV(6); WAITVM(3); LDV(7);
      asm volatile("buffer_load_dwordx4 %0, %2, %3, 0 offen sc1\n\tbuffer_load_dwordx4 %1, %2, %3, 0 offen offset:1024 sc1"
                   : "=&v"(fl0), "=&v"(fl1) : "v"(w == 7 ? WS_FLAG + lane * 16 : OOB), "s"(rs_ws) : "memory");
      WAITVM(3); LDV(8); WAITVM(3); LDV(9); WAITVM(3); LDV(10); WAITVM(3); LDV(11);
      WAITVM(3); LDV(12); WAITVM(3); LDV(13); WAITVM(3); LDV(14); WAITVM(3); LDV(15);
      if (role == 3) {
        const rsrc_t rs = mk_rsrc(ka->wo + (size_t)(1024 * g + 128 * w) * DM + 32 * r, 128 * 1280);
        const u32 vo = DMA_LANE(1280);
        asm volatile("s_nop 4" ::: "memory");
#pragma unroll
        for (int i = 0; i < 8; ++i) dma16(L_WR + w * 8192 + i * 1024, vo, rs, i * 16 * 1280);
      }
      STAMP(3);
      asm volatile("" : "+v"(p1), "+v"(fl0), "+v"(fl1));
      _Pragma("nounroll") for (u32 spin = 0; spin < SPIN_MAX; ++spin) {
        if (__builtin_amdgcn_ballot_w64(p1.z != tag_lo || p1.w != tag_hi) == 0ull) break;
        asm volatile("buffer_load_dwordx4 %0, %1, %2, 0 offen sc1\n\ts_waitcnt vmcnt(0)" : "=&v"(p1) : "v"(q1off), "s"(rs_ws) : "memory");
      }
      qv = v2u{p1.x, p1.y};
      if (w == 7) {
        _Pragma("nounroll") for (u32 spin = 0; spin < SPIN_MAX; ++spin) {
          const bool bad = fl0.x != tag_lo || fl0.y != tag_hi || fl0.z != tag_lo || fl0.w != tag_hi || fl1.x != tag_lo || fl1.y != tag_hi || fl1.z != tag_lo || fl1.w != tag_hi;
          if (__builtin_amdgcn_ballot_w64(bad) == 0ull) break;
          asm volatile("buffer_load_dwordx4 %0, %2, %3, 0 offen sc1\n\tbuffer_load_dwordx4 %1, %2, %3, 0 offen offset:1024 sc1\n\ts_waitcnt vmcnt(0)"
                       : "=&v"(fl0), "=&v"(fl1) : "v"(WS_FLAG + lane * 16), "s"(rs_ws) : "memory");
        }
      }
    } else {
      STAGE_VARS();
      const u32 off = S_Q2 + ((h * 8 + (lane >> 3)) * 4 + b) * 64 + (lane & 7) * 8;
      _Pragma("nounroll") for (u32 spin = 0; spin < SPIN_MAX; ++spin) {
        asm volatile("buffer_load_dwordx2 %0, %1, %2, 0 offen sc1\n\ts_waitcnt vmcnt(0)" : "=&v"(qv) : "v"(off), "s"(rs_ws) : "memory");
        if (all_valid(qv.x > qv.y ? qv.x : qv.y)) break;
      }
    }
    STAMP(4 + it * 12);
    const u32 pbase = it * PASS_STRIDE;
    {
      STAGE_VARS();
      {
        float q0 = bflo(qv.x), q1 = bfhi(qv.x), q2 = bflo(qv.y), q3 = bfhi(qv.y);
        q0 += dpp<ROR8>(q0); q1 += dpp<ROR8>(q1); q2 += dpp<ROR8>(q2); q3 += dpp<ROR8>(q3);
        *(LAS v2u*)(L + L_QB + w * 256 + g4 * 64 + (lane & 7) * 8) = v2u{cvtpk(q0, q1), cvtpk(q2, q3)};
      }
      asm volatile("s_waitcnt lgkmcnt(0)" ::: "memory");
      const v4u qa0 = *(LAS v4u*)(L + L_QB + w * 256 + (c16 & 1) * 64 + g4 * 16);
      const v4u qa1 = *(LAS v4u*)(L + L_QB + w * 256 + 128 + (c16 & 1) * 64 + g4 * 16);
#pragma unroll
      for (int i = 0; i < 16; ++i) asm volatile("" : "+a"(kr[i]));
      f4 sc[8];
#pragma unroll
      for (int t = 0; t < 8; ++t) {
        sc[t] = mfma(qa0, kr[(t >> 1) * 4 + (t & 1) * 2], f4{0, 0, 0, 0});
        sc[t] = mfma(qa1, kr[(t >> 1) * 4 + (t & 1) * 2 + 1], sc[t]);
      }
      float sa = sc[0][0], sb = sc[1][0];
      sa = sel<0x1, 0xC>(sa, sc[1][1]); sb = sel<0x1, 0xC>(sb, sc[0][1]);
      sa = sel<0x2, 0x3>(sa, sc[2][0]); sb = sel<0x2, 0x3>(sb, sc[3][0]);
      sa = sel<0x2, 0xC>(sa, sc[3][1]); sb = sel<0x2, 0xC>(sb, sc[2][1]);
      sa = sel<0x4, 0x3>(sa, sc[4][0]); sb = sel<0x4, 0x3>(sb, sc[5][0]);
      sa = sel<0x4, 0xC>(sa, sc[5][1]); sb = sel<0x4, 0xC>(sb, sc[4][1]);
      sa = sel<0x8, 0x3>(sa, sc[6][0]); sb = sel<0x8, 0x3>(sb, sc[7][0]);
      sa = sel<0x8, 0xC>(sa, sc[7][1]); sb = sel<0x8, 0xC>(sb, sc[6][1]);
      const float s2 = (sa + dpp<ROR8>(sb)) * (0.08838834764831845f * 1.4426950408889634f);
      const float mw = wave_max(s2);
      const float pe = __builtin_amdgcn_exp2f(s2 - mw);
      const float lw = wave_sum(pe);
      const u32 pp = cvtpk(pe, dpp<SHL1>(pe));
      float oc[8] = {0, 0, 0, 0, 0, 0, 0, 0};
#define VPAIR(m) do { _Pragma("unroll") for (int u = 0; u < 4; ++u) { u32 t_; \
      asm volatile("v_perm_b32 %2, %1, %0, %3\n\tv_perm_b32 %1, %1, %0, %4\n\tv_mov_b32 %0, %2" : "+v"(vr[2 * (m)][u]), "+v"(vr[2 * (m) + 1][u]), "=&v"(t_) : "s"(0x05040100u), "s"(0x07060302u)); } } while (0)
#define PV(m, bc) asm volatile("s_nop 1\n\t" \
      "v_dot2c_f32_bf16_dpp %0, %8, %9 row_newbcast:" #bc " row_mask:0xf bank_mask:0xf\n\t" \
      "v_dot2c_f32_bf16_dpp %1, %8, %13 row_newbcast:" #bc " row_mask:0xf bank_mask:0xf\n\t" \
      "v_dot2c_f32_bf16_dpp %2, %8, %10 row_newbcast:" #bc " row_mask:0xf bank_mask:0xf\n\t" \
      "v_dot2c_f32_bf16_dpp %3, %8, %14 row_newbcast:" #bc " row_mask:0xf bank_mask:0xf\n\t" \
      "v_dot2c_f32_bf16_dpp %4, %8, %11 row_newbcast:" #bc " row_mask:0xf bank_mask:0xf\n\t" \
      "v_dot2c_f32_bf16_dpp %5, %8, %15 row_newbcast:" #bc " row_mask:0xf bank_mask:0xf\n\t" \
      "v_dot2c_f32_bf16_dpp %6, %8, %12 row_newbcast:" #bc " row_mask:0xf bank_mask:0xf\n\t" \
      "v_dot2c_f32_bf16_dpp %7, %8, %16 row_newbcast:" #bc " row_mask:0xf bank_mask:0xf\n\ts_nop 2" \
      : "+v"(oc[0]), "+v"(oc[1]), "+v"(oc[2]), "+v"(oc[3]), "+v"(oc[4]), "+v"(oc[5]), "+v"(oc[6]), "+v"(oc[7]) \
      : "v"(pp), "v"(vr[2 * (m)][0]), "v"(vr[2 * (m)][1]), "v"(vr[2 * (m)][2]), "v"(vr[2 * (m)][3]), \
        "v"(vr[2 * (m) + 1][0]), "v"(vr[2 * (m) + 1][1]), "v"(vr[2 * (m) + 1][2]), "v"(vr[2 * (m) + 1][3]))
      if (it == 0) {
#pragma unroll
        for (int i = 0; i < 12; ++i) asm volatile("" : "+v"(vr[i]));
        VPAIR(0); VPAIR(1); VPAIR(2); VPAIR(3); VPAIR(4); VPAIR(5);
      }
      PV(0, 0); PV(1, 2); PV(2, 4); PV(3, 6); PV(4, 8); PV(5, 10);
      if (it == 0) {
        if (role == 3) WAITVM(10); else WAITVM(2);
        asm volatile("" : "+v"(vr[12]), "+v"(vr[13]));
        VPAIR(6);
      }
      PV(6, 12);
      if (it == 0) {
        if (role == 3) WAITVM(8); else WAITVM(0);
        asm volatile("" : "+v"(vr[14]), "+v"(vr[15]));
        VPAIR(7);
      }
      PV(7, 14);
      STAMP(5 + it * 12);
      float t4[4], u2[2];
#pragma unroll
      for (int d = 0; d < 4; ++d) {
        auto s = __builtin_amdgcn_permlane32_swap(__float_as_uint(oc[d]), __float_as_uint(oc[d + 4]), false, false);
        t4[d] = __uint_as_float(s[0]) + __uint_as_float(s[1]);
      }
#pragma unroll
      for (int d = 0; d < 2; ++d) {
        auto s = __builtin_amdgcn_permlane16_swap(__float_as_uint(t4[d]), __float_as_uint(t4[d + 2]), false, false);
        u2[d] = __uint_as_float(s[0]) + __uint_as_float(s[1]);
      }
      *(LAS f2*)(L + L_RA + w * 512 + c16 * 32 + g4 * 8) = f2{u2[0], u2[1]};
      if (lane == 0) *(LAS f2*)(L + L_RA + 4096 + w * 8) = f2{mw, lw};
      lds_barrier();
      if (w == 0) {
        f2 st[8];
#pragma unroll
        for (int i = 0; i < 8; ++i) st[i] = *(LAS f2*)(L + L_RA + 4096 + i * 8);
        float M = st[0].x;
#pragma unroll
        for (int i = 1; i < 8; ++i) M = fmaxf(M, st[i].x);
        float Lsum = 0.f, o0 = 0.f, o1 = 0.f;
#pragma unroll
        for (int i = 0; i < 8; ++i) {
          const float f = __builtin_amdgcn_exp2f(st[i].x - M);
          const f2 v = *(LAS f2*)(L + L_RA + i * 512 + lane * 8);
          Lsum = fmaf(f, st[i].y, Lsum); o0 = fmaf(f, v.x, o0); o1 = fmaf(f, v.y, o1);
        }
        const float inv = __builtin_amdgcn_rcpf(Lsum);
        const u32 d = nosent(cvtpk(o0 * inv, o1 * inv));
        st16_sc1(quad_gather(d), (lane & 3) ? OOB : S_O0 + pbase + ((h * 2 + half) * 4 + b) * 256 + lane * 4, rs_ws);
        st8_sc1(v2u{nosent(__float_as_uint(M)), nosent(__float_as_uint(Lsum))}, lane == 0 ? S_OST0 + pbase + (h * 4 + b) * 16 + half * 8 : OOB, rs_ws);
      }
      STAMP(6 + it * 12);
    }
    if (role == 3) {
      STAGE_VARS();
      const int hh = 8 * g + w;
      const u32 o0off = S_O0 + pbase + (hh * 8 + (c16 & 3)) * 256 + (c16 >> 2) * 64 + g4 * 16;
      const u32 stoff = S_OST0 + pbase + (hh * 4 + (c16 & 3)) * 16;
      v4u oa, ob, st;
      asm volatile("buffer_load_dwordx4 %0, %3, %5, 0 offen sc1\n\tbuffer_load_dwordx4 %1, %3, %5, 0 offen offset:1024 sc1\n\tbuffer_load_dwordx4 %2, %4, %5, 0 offen sc1\n\ts_waitcnt vmcnt(3)"
                   : "=&v"(oa), "=&v"(ob), "=&v"(st) : "v"(o0off), "v"(stoff), "s"(rs_ws) : "memory");
      v4u wf[8];
      const u32 wf0 = WF0();
#pragma unroll
      for (int s = 0; s < 4; ++s)
#pragma unroll
        for (int t = 0; t < 2; ++t) wf[s * 2 + t] = wfrag(L + L_WR + w * 8192 + s * 2048 + (wf0 ^ (t * 32)));
      asm volatile("s_waitcnt vmcnt(0)" : "+v"(oa), "+v"(ob), "+v"(st)::"memory");
      _Pragma("nounroll") for (u32 spin = 0; spin < SPIN_MAX; ++spin) {
        if (all_valid(umax3(umax4(oa), umax4(ob), umax4(st)))) break;
        asm volatile("buffer_load_dwordx4 %0, %3, %5, 0 offen sc1\n\tbuffer_load_dwordx4 %1, %3, %5, 0 offen offset:1024 sc1\n\tbuffer_load_dwordx4 %2, %4, %5, 0 offen sc1\n\ts_waitcnt vmcnt(0)"
                     : "=&v"(oa), "=&v"(ob), "=&v"(st) : "v"(o0off), "v"(stoff), "s"(rs_ws) : "memory");
      }
      STAMP(7 + it * 12);
      const float m0 = __uint_as_float(st.x), l0 = __uint_as_float(st.y), m1 = __uint_as_float(st.z), l1 = __uint_as_float(st.w);
      const float mx = fmaxf(m0, m1);
      const float w0 = l0 * __builtin_amdgcn_exp2f(m0 - mx), w1 = l1 * __builtin_amdgcn_exp2f(m1 - mx);
      const float inv = __builtin_amdgcn_rcpf(w0 + w1), a0 = w0 * inv, a1 = w1 * inv;
      v4u of;
#pragma unroll
      for (int j = 0; j < 4; ++j) of[j] = cvtpk(fmaf(a0, bflo(oa[j]), a1 * bflo(ob[j])), fmaf(a0, bfhi(oa[j]), a1 * bfhi(ob[j])));
      f4 acc[8];
#pragma unroll
      for (int i = 0; i < 8; ++i) acc[i] = mfma(wf[i], of, f4{0, 0, 0, 0});
#pragma unroll
      for (int t = 0; t < 2; ++t) {
        f4 y;
#pragma unroll
        for (int j = 0; j < 4; ++j) y[j] = acc[t][j] + dpp<SHL4>(acc[2 + t][j]) + dpp<SHL8>(acc[4 + t][j]) + dpp<SHL12>(acc[6 + t][j]);
        if (c16 < 4) *(LAS f4*)(L + L_RR + w * 512 + t * 256 + c16 * 64 + g4 * 16) = y;
      }
      lds_barrier();
      if (w == 0) {
        f2 s = red_sum<8, 1>(L + L_RR, lane);
        if (g == 0) { const u32 x = *(LAS u32*)(L + L_XR + g4 * 1280 + (32 * r + 2 * c16) * 2); s.x += bflo(x); s.y += bfhi(x); }
        const u32 d = nosent(cvtpk(s.x, s.y));
        st16_sc1(quad_gather(d), (lane & 3) < 2 ? S_X1P0 + pbase + (lane & 3) * 20480 + (g * 20 + r) * 256 + (lane >> 2) * 16 : OOB, rs_ws);
      }
    } else if (role == 4) {
      STAGE_VARS();
      const int j4 = (g - 4) * 27 + r;
      if (it == 0) {
        lds_barrier();
        const rsrc_t rs = mk_rsrc(((w & 1) ? ka->wu : ka->wg) + (size_t)(160 * (w >> 1)) * FF + 32 * j4, 160 * 3456);
        const u32 vo = DMA_LANE(3456);
        asm volatile("s_nop 4" ::: "memory");
#pragma unroll
        for (int i = 0; i < 10; ++i) dma16(L_WR + w * 10240 + i * 1024, vo, rs, i * 16 * 3456);
        WAITVM(0);
      } else {
        idle<SLP_S4>();
      }
      v4u wf[10];
      const u32 wf0 = WF0();
#pragma unroll
      for (int s = 0; s < 5; ++s)
#pragma unroll
        for (int t = 0; t < 2; ++t) wf[s * 2 + t] = wfrag(L + L_WR + w * 10240 + s * 2048 + (wf0 ^ (t * 32)));
#pragma unroll
      for (int i = 0; i < 10; ++i) asm volatile("" : "+a"(wf[i]));
      if (w < 4) {
        const u32 base = S_X1P0 + pbase + (g - 4) * 20480 + w * 64;
        const u32 o1 = base + (lane >> 2) * 256 + (lane & 3) * 16, o2 = lane < 16 ? base + (16 + (lane >> 2)) * 256 + (lane & 3) * 16 : OOB;
        v4u pa[4], pb[4];
        _Pragma("nounroll") for (u32 spin = 0; spin < SPIN_MAX; ++spin) {
          asm volatile("buffer_load_dwordx4 %0, %8, %10, 0 offen sc1\n\tbuffer_load_dwordx4 %1, %9, %10, 0 offen sc1\n\t"
                       "buffer_load_dwordx4 %2, %8, %10, %11 offen sc1\n\tbuffer_load_dwordx4 %3, %9, %10, %11 offen sc1\n\t"
                       "buffer_load_dwordx4 %4, %8, %10, %12 offen sc1\n\tbuffer_load_dwordx4 %5, %9, %10, %12 offen sc1\n\t"
                       "buffer_load_dwordx4 %6, %8, %10, %13 offen sc1\n\tbuffer_load_dwordx4 %7, %9, %10, %13 offen sc1\n\ts_waitcnt vmcnt(0)"
                       : "=&v"(pa[0]), "=&v"(pb[0]), "=&v"(pa[1]), "=&v"(pb[1]), "=&v"(pa[2]), "=&v"(pb[2]), "=&v"(pa[3]), "=&v"(pb[3])
                       : "v"(o1), "v"(o2), "s"(rs_ws), "s"(5120u), "s"(10240u), "s"(15360u) : "memory");
          u32 mx = 0;
#pragma unroll
          for (int q = 0; q < 4; ++q) mx = umax3(mx, umax4(pa[q]), umax4(pb[q]));
          if (all_valid(mx)) break;
        }
        STAMP(8 + it * 12);
        float fa[8], fb[8];
#pragma unroll
        for (int j = 0; j < 4; ++j) {
          fa[2 * j] = (bflo(pa[0][j]) + bflo(pa[1][j])) + (bflo(pa[2][j]) + bflo(pa[3][j]));
          fa[2 * j + 1] = (bfhi(pa[0][j]) + bfhi(pa[1][j])) + (bfhi(pa[2][j]) + bfhi(pa[3][j]));
          fb[2 * j] = (bflo(pb[0][j]) + bflo(pb[1][j])) + (bflo(pb[2][j]) + bflo(pb[3][j]));
          fb[2 * j + 1] = (bfhi(pb[0][j]) + bfhi(pb[1][j])) + (bfhi(pb[2][j]) + bfhi(pb[3][j]));
        }
        ln_row(fa, fb, lane, L + L_XN + w * XN_STRIDE);
      }
      lds_barrier();
      f4 acc0 = {0, 0, 0, 0}, acc1 = {0, 0, 0, 0};
#pragma unroll
      for (int s = 0; s < 5; ++s) {
        const v4u xf = *(LAS v4u*)(L + L_XN + (c16 & 3) * XN_STRIDE + ((w >> 1) * 160 + s * 32 + 8 * g4) * 2);
        acc0 = mfma(wf[s * 2], xf, acc0);
        acc1 = mfma(wf[s * 2 + 1], xf, acc1);
      }
      if (c16 < 4) {
        *(LAS f4*)(L + L_RR + w * 512 + c16 * 64 + g4 * 16) = acc0;
        *(LAS f4*)(L + L_RR + w * 512 + 256 + c16 * 64 + g4 * 16) = acc1;
      }
      lds_barrier();
      if (w == 0) {
        const f2 ga = red_sum<4, 2>(L + L_RR, lane), up = red_sum<4, 2>(L + L_RR + 512, lane);
        const u32 d = nosent(cvtpk((ga.x > 0.f ? ga.x : 0.f * ga.x) * up.x, (ga.y > 0.f ? ga.y : 0.f * ga.y) * up.y));
        st16_sc1(quad_gather(d), (lane & 3) ? OOB : S_HB0 + pbase + j4 * 256 + lane * 4, rs_ws);
      }
    } else if (role == 5) {
      STAGE_VARS();
      const int s0 = w < 6 ? 7 * w : 42 + 6 * (w - 6);
      if (it == 0) {
        lds_barrier();
        const rsrc_t rs = mk_rsrc(ka->wd + (size_t)(32 * s0) * DM + 32 * r, (w < 6 ? 224 : 192) * 1280);
        const u32 vo = DMA_LANE(1280);
        asm volatile("s_nop 4" ::: "memory");
#pragma unroll
        for (int i = 0; i < 12; ++i) dma16(L_WR + s0 * 2048 + i * 1024, vo, rs, i * 16 * 1280);
        if (w < 6) { dma16(L_WR + s0 * 2048 + 12 * 1024, vo, rs, 12 * 16 * 1280); dma16(L_WR + s0 * 2048 + 13 * 1024, vo, rs, 13 * 16 * 1280); }
        WAITVM(0);
      }
      idle<SLP_S5>();
      v4u wf[14];
      const u32 wf0 = WF0();
#pragma unroll
      for (int s = 0; s < 6; ++s)
#pragma unroll
        for (int t = 0; t < 2; ++t) wf[s * 2 + t] = wfrag(L + L_WR + (s0 + s) * 2048 + (wf0 ^ (t * 32)));
      {
        const int s6 = w < 6 ? s0 + 6 : s0;
        wf[12] = wfrag(L + L_WR + s6 * 2048 + wf0); wf[13] = wfrag(L + L_WR + s6 * 2048 + (wf0 ^ 32));
      }
#pragma unroll
      for (int i = 0; i < 14; ++i) asm volatile("" : "+a"(wf[i]));
      const int slot = c16 >> 2;
      const u32 hoff = S_HB0 + pbase + (c16 & 3) * 64 + g4 * 16;
      const u32 oA = hoff + (s0 + slot) * 256;
      const u32 oB = (slot < (w < 6 ? 3 : 2)) ? hoff + (s0 + 4 + slot) * 256 : OOB;
      v4u ha, hb;
      u32 rx0, rx1, rx2, rx3;
      const u32 rxo = w == 7 ? S_X1P0 + pbase + r * 256 + lane * 4 : OOB;
      _Pragma("nounroll") for (u32 spin = 0; spin < SPIN_MAX; ++spin) {
        asm volatile("buffer_load_dwordx4 %0, %6, %9, 0 offen sc1\n\tbuffer_load_dwordx4 %1, %7, %9, 0 offen sc1\n\t"
                     "buffer_load_dword %2, %8, %9, 0 offen sc1\n\tbuffer_load_dword %3, %8, %9, %10 offen sc1\n\tbuffer_load_dword %4, %8, %9, %11 offen sc1\n\tbuffer_load_dword %5, %8, %9, %12 offen sc1\n\ts_waitcnt vmcnt(0)"
                     : "=&v"(ha), "=&v"(hb), "=&v"(rx0), "=&v"(rx1), "=&v"(rx2), "=&v"(rx3)
                     : "v"(oA), "v"(oB), "v"(rxo), "s"(rs_ws), "s"(5120u), "s"(10240u), "s"(15360u) : "memory");
        if (all_valid(umax3(umax3(umax4(ha), umax4(hb), rx0), umax3(rx1, rx2, rx3), 0u))) break;
      }
      STAMP(9 + it * 12);
      f4 acc[8];
#pragma unroll
      for (int i = 0; i < 8; ++i) acc[i] = mfma(wf[i], ha, f4{0, 0, 0, 0});
#pragma unroll
      for (int i = 0; i < 4; ++i) acc[i] = mfma(wf[8 + i], hb, acc[i]);
      if (w < 6) { acc[4] = mfma(wf[12], hb, acc[4]); acc[5] = mfma(wf[13], hb, acc[5]); }
#pragma unroll
      for (int t = 0; t < 2; ++t) {
        f4 y;
#pragma unroll
        for (int j = 0; j < 4; ++j) y[j] = acc[t][j] + dpp<SHL4>(acc[2 + t][j]) + dpp<SHL8>(acc[4 + t][j]) + dpp<SHL12>(acc[6 + t][j]);
        if (c16 < 4) *(LAS f4*)(L + L_RR + w * 512 + t * 256 + c16 * 64 + g4 * 16) = y;
      }
      if (w == 7)
        *(LAS f2*)(L + L_RR + 8 * 512 + ((lane >> 3) & 1) * 256 + g4 * 64 + (lane & 7) * 8) =
            f2{(bflo(rx0) + bflo(rx1)) + (bflo(rx2) + bflo(rx3)), (bfhi(rx0) + bfhi(rx1)) + (bfhi(rx2) + bfhi(rx3))};
      lds_barrier();
      if (w == 0) {
        const f2 s = red_sum<9, 1>(L + L_RR, lane);
        const u32 d0 = cvtpk(s.x, s.y);
        if (it == 0) {
          const v4u q = quad_gather(nosent(d0));
          const u32 off = S_X2 + (lane & 3) * 5120 + r * 256 + (lane >> 2) * 16;
          st16_sc1(q, off, rs_ws);
          st16_sc1(q, off + 4 * 5120, rs_ws);
        } else {
          const v4u q = quad_gather(d0);
          const rsrc_t rs_o = mk_rsrc(outp, 4 * DM * 2);
          asm volatile("s_nop 4\n\tbuffer_store_dwordx4 %0, %1, %2, 0 offen\n\ts_nop 1" ::"v"(q), "v"((lane & 3) ? OOB : (u32)(g4 * 1280 + r * 64 + (c16 >> 2) * 16)), "s"(rs_o) : "memory");
          if (r == 0) st8_sc1(v2u{tag_lo, tag_hi}, lane == 0 ? WS_G : OOB, rs_ws);
        }
      }
    }
    STAMP(10 + it * 12);
  }
  STAMP_OUT();
  asm volatile(".Lfd_code_end:");
}

}

extern "C" void kernel_launch(void* const* d_in, const int* in_sizes, int n_in, void* d_out, int out_size, void* d_ws,
                              size_t ws_size, hipStream_t stream) {
  Args a;
  a.x = (const u16*)d_in[0];
  a.k = (const u16*)d_in[1];
  a.v = (const u16*)d_in[2];
  a.wq = (const u16*)d_in[3];
  a.wo = (const u16*)d_in[4];
  a.wg = (const u16*)d_in[6];
  a.wu = (const u16*)d_in[7];
  a.wd = (const u16*)d_in[8];
  a.out = (u16*)d_out;
  a.ws = (unsigned char*)d_ws;
  (void)hipFuncSetAttribute((const void*)fused_decode, hipFuncAttributeMaxDynamicSharedMemorySize, (int)L_END);
  hipLaunchKernelGGL(fused_decode, dim3(NWG), dim3(NT), L_END, stream, a);
}
```

```cpp
#include <hip/hip_runtime.h>
#include <hip/hip_bf16.h>

typedef unsigned int u32;
typedef unsigned long long u64;
typedef unsigned short u16;
typedef u32 v4u __attribute__((ext_vector_type(4)));
typedef u32 v2u __attribute__((ext_vector_type(2)));
typedef float f4 __attribute__((ext_vector_type(4)));
typedef float f2 __attribute__((ext_vector_type(2)));
typedef __bf16 bf16x8 __attribute__((ext_vector_type(8)));
typedef short s16x4 __attribute__((ext_vector_type(4)));
#define LAS __attribute__((address_space(3)))

namespace {

constexpr int NWG = 256, NT = 512;
constexpr int DM = 640, SK = 4096, WIN = 1024, DH = 128, FF = 1728;

constexpr u32 WS_G = 0;
constexpr u32 WS_FLAG = 4096;
constexpr u32 WS_Q1 = 8192;
constexpr u32 WS_S = 262144;
constexpr u32 S_O0 = WS_S, S_OST0 = S_O0 + 65536, S_X1P0 = S_OST0 + 2048, S_HB0 = S_X1P0 + 40960, S_X2 = S_HB0 + 13824;
constexpr u32 S_O1 = S_X2 + 40960;
constexpr u32 PASS_STRIDE = S_O1 - S_O0;
constexpr u32 S_Q2 = S_O1 + (S_X2 - S_O0);
constexpr u32 WS_END = WS_S + 256 * 1536;
static_assert(S_Q2 + 65536 <= WS_END, "sentinel region too small");
static_assert((S_X2 % 128) == 0 && (S_O1 % 128) == 0 && (S_Q2 % 128) == 0 && (S_HB0 % 128) == 0, "alignment");
constexpr u32 WS_BYTES = 4u << 20;
constexpr u32 OOB = 0x80000000u;

constexpr u32 L_WR = 0;
constexpr u32 L_WQ = 110592;
constexpr u32 L_XN = 131072;
constexpr u32 XN_STRIDE = 1344;
constexpr u32 L_XR = L_XN + 4 * XN_STRIDE;
constexpr u32 L_RA = L_XR + 5120;
constexpr u32 L_RR = L_RA + 4096 + 128;
constexpr u32 L_RQ = L_RR + 9 * 512;
constexpr u32 L_QB = L_RQ + 5 * 512;
constexpr u32 L_END = L_QB + 2048;
static_assert(L_END <= 160 * 1024 && L_END > 80 * 1024, "LDS");

constexpr u32 SENT = 0xFFFFFFFFu;
constexpr u32 SPIN_MAX = 1u << 16;

#define SLP_S4 20
#define SLP_S5 50
#define SLP_X_NONE 90
#define SLP_X_S3 55
#define SLP_X_S4 20

struct Args {
  const u16* x; const u16* k; const u16* v; const u16* wq; const u16* wo;
  const u16* wg; const u16* wu; const u16* wd; u16* out; unsigned char* ws;
};

typedef __amdgpu_buffer_rsrc_t rsrc_t;
__device__ __forceinline__ rsrc_t mk_rsrc(const void* p, u32 bytes) { return __builtin_amdgcn_make_buffer_rsrc((void*)p, 0, (int)bytes, 0x00020000); }

__device__ __forceinline__ float bflo(u32 d) { return __builtin_bit_cast(float, d << 16); }
__device__ __forceinline__ float bfhi(u32 d) { return __builtin_bit_cast(float, d & 0xffff0000u); }
__device__ __forceinline__ u32 cvtpk(float lo, float hi) {
  typedef __bf16 bf16x2 __attribute__((ext_vector_type(2)));
  typedef float f32x2 __attribute__((ext_vector_type(2)));
  f32x2 v = {lo, hi};
  return __builtin_bit_cast(u32, __builtin_convertvector(v, bf16x2));
}
__device__ __forceinline__ u32 nosent(u32 d) { return d < 0xFFFFFFFEu ? d : 0xFFFFFFFEu; }
template <int CTRL> __device__ __forceinline__ float dpp(float x) {
  return __builtin_bit_cast(float, __builtin_amdgcn_mov_dpp(__builtin_bit_cast(int, x), CTRL, 0xf, 0xf, true));
}
template <int CTRL> __device__ __forceinline__ u32 dppu(u32 x) { return (u32)__builtin_amdgcn_mov_dpp((int)x, CTRL, 0xf, 0xf, true); }
template <int RM, int BM> __device__ __forceinline__ float sel(float old, float src) {
  return __builtin_bit_cast(float, __builtin_amdgcn_update_dpp(__builtin_bit_cast(int, old), __builtin_bit_cast(int, src), 0xE4, RM, BM, false));
}
constexpr int ROR8 = 0x128, ROR4 = 0x124, ROR2 = 0x122, ROR1 = 0x121, SHL1 = 0x101, SHL4 = 0x104, SHL8 = 0x108, SHL12 = 0x10C;
__device__ __forceinline__ float wave_sum(float x) {
  x += dpp<ROR8>(x); x += dpp<ROR4>(x); x += dpp<ROR2>(x); x += dpp<ROR1>(x);
  auto s = __builtin_amdgcn_permlane16_swap(__float_as_uint(x), __float_as_uint(x), false, false);
  x = __uint_as_float(s[0]) + __uint_as_float(s[1]);
  auto t = __builtin_amdgcn_permlane32_swap(__float_as_uint(x), __float_as_uint(x), false, false);
  return __uint_as_float(t[0]) + __uint_as_float(t[1]);
}
__device__ __forceinline__ float wave_max(float x) {
  x = fmaxf(x, dpp<ROR8>(x)); x = fmaxf(x, dpp<ROR4>(x)); x = fmaxf(x, dpp<ROR2>(x)); x = fmaxf(x, dpp<ROR1>(x));
  auto s = __builtin_amdgcn_permlane16_swap(__float_as_uint(x), __float_as_uint(x), false, false);
  x = fmaxf(__uint_as_float(s[0]), __uint_as_float(s[1]));
  auto t = __builtin_amdgcn_permlane32_swap(__float_as_uint(x), __float_as_uint(x), false, false);
  return fmaxf(__uint_as_float(t[0]), __uint_as_float(t[1]));
}
__device__ __forceinline__ f4 mfma(v4u a, v4u b, f4 c) {
  return __builtin_amdgcn_mfma_f32_16x16x32_bf16(__builtin_bit_cast(bf16x8, a), __builtin_bit_cast(bf16x8, b), c, 0, 0, 0);
}
__device__ __forceinline__ u32 umax3(u32 a, u32 b, u32 c) { u32 m = a > b ? a : b; return m > c ? m : c; }
__device__ __forceinline__ u32 umax4(v4u v) { return umax3(v.x, v.y, v.z > v.w ? v.z : v.w); }
__device__ __forceinline__ bool all_valid(u32 mx) { return __builtin_amdgcn_ballot_w64(mx == SENT) == 0ull; }
__device__ __forceinline__ void lds_barrier() {
  asm volatile("s_waitcnt lgkmcnt(0)" ::: "memory");
  __builtin_amdgcn_s_barrier();
  asm volatile("" ::: "memory");
}
__device__ __forceinline__ v4u wfrag(LAS unsigned char* p) {
  typedef short v4i16_t __attribute__((ext_vector_type(4)));
  v4i16_t lo = __builtin_amdgcn_ds_read_tr16_b64_v4i16((LAS v4i16_t*)p);
  v4i16_t hi = __builtin_amdgcn_ds_read_tr16_b64_v4i16((LAS v4i16_t*)(p + 256));
  v2u a = __builtin_bit_cast(v2u, lo), b = __builtin_bit_cast(v2u, hi);
  return v4u{a.x, a.y, b.x, b.y};
}
#define WAITVM(n) asm volatile("s_waitcnt vmcnt(" #n ")" ::: "memory")
__device__ __forceinline__ void dma16(u32 ldsaddr, u32 voff, rsrc_t rs, u32 soff) {
  asm volatile("s_mov_b32 m0, %0\n\ts_nop 0\n\tbuffer_load_dwordx4 %1, %2, %3 offen nt lds" ::"s"(ldsaddr), "v"(voff), "s"(rs), "s"(soff) : "memory");
}
__device__ __forceinline__ void st16_sc1(v4u d, u32 voff, rsrc_t rs) {
  asm volatile("buffer_store_dwordx4 %0, %1, %2, 0 offen sc1\n\ts_nop 1" ::"v"(d), "v"(voff), "s"(rs) : "memory");
}
__device__ __forceinline__ void st8_sc1(v2u d, u32 voff, rsrc_t rs) {
  asm volatile("buffer_store_dwordx2 %0, %1, %2, 0 offen sc1\n\ts_nop 1" ::"v"(d), "v"(voff), "s"(rs) : "memory");
}
__device__ __forceinline__ v4u quad_gather(u32 d) {
  return v4u{dppu<0x00>(d), dppu<0x55>(d), dppu<0xAA>(d), dppu<0xFF>(d)};
}
template <int N> __device__ __forceinline__ void idle() {
  if (N > 0) __builtin_amdgcn_s_sleep(N > 127 ? 127 : N);
}

#define LAUNDER_V(x) asm volatile("" : "+v"(x))
#define LAUNDER_S(x) asm volatile("" : "+s"(x))
typedef const __attribute__((address_space(4))) Args* kargs_t;
#define STAGE_VARS() int lane = lane_; LAUNDER_V(lane); const int g4 = lane >> 4, c16 = lane & 15; (void)g4; (void)c16; \
    int w = w_, c = c_; LAUNDER_S(w); LAUNDER_S(c); const int g = c & 7, r = c >> 3; (void)g; (void)r; \
    const int h = 4 * g + (r >> 3), bh = r & 7, b = bh >> 1, half = bh & 1; (void)h; (void)b; (void)half; \
    kargs_t ka = ka_; LAUNDER_S(ka); (void)ka

#define STAMP(i) do { } while (0)
#define STAMP_OUT() do { } while (0)

__device__ __forceinline__ void ln_row(const float (&xa)[8], const float (&xb)[8], int lane, LAS unsigned char* dst) {
  const float sh = __builtin_bit_cast(float, __builtin_amdgcn_readfirstlane(__builtin_bit_cast(int, xa[0])));
  const float mb = lane < 16 ? 1.f : 0.f;
  float s1 = 0.f, s2 = 0.f, t1 = 0.f, t2 = 0.f;
#pragma unroll
  for (int j = 0; j < 8; ++j) {
    const float d = xa[j] - sh, e = xb[j] - sh;
    s1 += d; s2 = fmaf(d, d, s2); t1 += e; t2 = fmaf(e, e, t2);
  }
  s1 = wave_sum(fmaf(mb, t1, s1));
  s2 = wave_sum(fmaf(mb, t2, s2));
  const float md = s1 * (1.f / DM);
  const float var = fmaxf(fmaf(-md, md, s2 * (1.f / DM)), 0.f);
  const float rstd = __builtin_amdgcn_rsqf(var + 1e-5f);
  const float mean = sh + md;
  v4u oa, ob;
#pragma unroll
  for (int j = 0; j < 4; ++j) {
    oa[j] = cvtpk((xa[2 * j] - mean) * rstd, (xa[2 * j + 1] - mean) * rstd);
    ob[j] = cvtpk((xb[2 * j] - mean) * rstd, (xb[2 * j + 1] - mean) * rstd);
  }
  *(LAS v4u*)(dst + lane * 16) = oa;
  if (lane < 16) *(LAS v4u*)(dst + 1024 + lane * 16) = ob;
}

template <int NP, int STEP> __device__ __forceinline__ f2 red_sum(LAS unsigned char* base, int lane) {
  LAS unsigned char* p = base + ((lane >> 3) & 1) * 256 + (lane >> 4) * 64 + (lane & 7) * 8;
  f2 s = *(LAS f2*)p;
#pragma unroll
  for (int i = 1; i < NP; ++i) s += *(LAS f2*)(p + i * STEP * 512);
  return s;
}

__global__ void __launch_bounds__(NT, 2) fused_decode(Args a_) {
  extern __shared__ __attribute__((aligned(16))) unsigned char smem_[];
  LAS unsigned char* const L = (LAS unsigned char*)smem_;
  const int lane_ = threadIdx.x & 63;
  const int w_ = __builtin_amdgcn_readfirstlane(threadIdx.x >> 6);
  const int c_ = blockIdx.x;
  const kargs_t ka_ = (kargs_t)__builtin_amdgcn_kernarg_segment_ptr();
  const int role = ((c_ & 7) < 4 && (c_ >> 3) < 20) ? 3 : (((c_ & 7) == 4 || (c_ & 7) == 5) && (c_ >> 3) < 27) ? 4 : ((c_ & 7) == 6 && (c_ >> 3) < 20) ? 5 : 0;
  const rsrc_t rs_ws = mk_rsrc(a_.ws, WS_BYTES);
  u16* const outp = a_.out;

  STAMP(11);
  v4u kr[16], vr[16];
  v4u xa, xb;
  u32 tag_lo, tag_hi;
  u32 tch0 = 0, tch1 = 0, tch2 = 0;
#define KV_DESC() const size_t kvoff = ((size_t)(b * 32 + h) * SK + (SK - WIN) + half * 512 + w * 64) * DH; \
  const rsrc_t rs_k = mk_rsrc(ka->k + kvoff, 16384), rs_v = mk_rsrc(ka->v + kvoff, 16384); (void)rs_v; \
  const u32 voK0 = (lane & 7) * 1024 + ((lane >> 3) & 1) * 64 + g4 * 16, voK1 = voK0 + 8192, voV = lane * 16; (void)voV
#define LDK(i) asm volatile("buffer_load_dwordx4 %0, %1, %2, 0 offen offset:%3 nt" : "+a"(kr[i]) : "v"((((i) >> 1) & 1) ? voK1 : voK0), "s"(rs_k), "n"(((i) >> 2) * 256 + ((i) & 1) * 128) : "memory")
#define LDV(i) asm volatile("buffer_load_dwordx4 %0, %1, %2, %3 offen nt" : "+v"(vr[i]) : "v"(voV), "s"(rs_v), "s"((u32)((i) * 1024)) : "memory")
#define DMA_LANE(stride) ((u32)(lane >> 2) * (stride) + (u32)(((lane & 3) ^ (((lane >> 5) & 1) << 1)) * 16))
#define WF0() ((u32)((8 * g4 + (c16 >> 2)) * 64 + ((((c16 & 3) >> 1) ^ ((g4 & 1) << 1)) * 16) + (c16 & 1) * 8))

  {
    STAGE_VARS();
    KV_DESC();
    asm volatile("s_nop 4" ::: "memory");
    v4u junk = {0, 0, 0, 0};
    {
      u64 pc; u32 len;
      asm volatile("s_getpc_b64 %0\n.Lfd_code_anchor:\n\ts_mov_b32 %1, .Lfd_code_end-.Lfd_code_anchor" : "=s"(pc), "=s"(len));
      const rsrc_t rs_c = mk_rsrc((const void*)pc, len);
      asm volatile("s_nop 4\n\tbuffer_load_dwordx4 %0, %1, %2, 0 offen" : "=v"(junk) : "v"(w == 7 ? (u32)(r * 1024 + lane * 16) : OOB), "s"(rs_c) : "memory");
    }
    const v4u sv = {SENT, SENT, SENT, SENT};
    if (w < 2) st16_sc1(sv, (w * 64 + lane) < 96 ? WS_S + c * 1536 + (w * 64 + lane) * 16 : OOB, rs_ws);
    v2u gen;
    asm volatile("buffer_load_dwordx2 %0, %1, %2, 0 offen sc1" : "=v"(gen) : "v"(WS_G), "s"(rs_ws) : "memory");
    const rsrc_t rs_x = mk_rsrc(ka->x, 4 * DM * 2);
    const u32 o1 = w < 4 ? w * 1280 + lane * 16 : OOB, o2 = (w < 4 && lane < 16) ? w * 1280 + 1024 + lane * 16 : OOB;
    asm volatile("s_nop 4\n\tbuffer_load_dwordx4 %0, %2, %4, 0 offen\n\tbuffer_load_dwordx4 %1, %3, %4, 0 offen"
                 : "=&v"(xa), "=&v"(xb) : "v"(o1), "v"(o2), "s"(rs_x) : "memory");
    if (w >= 3) {
      const rsrc_t rs = mk_rsrc(ka->wq + (size_t)(320 * (bh & 1) + 64 * (w - 3)) * 4096 + h * 128 + 32 * (bh >> 1), 64 * 8192);
      const u32 vo = DMA_LANE(8192);
#pragma unroll
      for (int i = 0; i < 4; ++i) dma16(L_WQ + (w - 3) * 4096 + i * 1024, vo, rs, i * 16 * 8192);
    }
#pragma unroll
    for (int i = 0; i < 16; ++i) { asm volatile("" : "=a"(kr[i])); asm volatile("" : "=v"(vr[i])); }
    LDK(0); LDK(1); LDK(2); LDK(3); LDK(4); LDK(5);
    {
      const u16* wp = role == 3 ? ka->wo + (size_t)(1024 * g + 128 * w) * DM : role == 4 ? ((w & 1) ? ka->wu : ka->wg) + (size_t)(160 * (w >> 1)) * FF
                    : role == 5 ? ka->wd + (size_t)(216 * w) * DM : ka->wo;
      const u32 stride = role == 4 ? 3456 * 2 : 1280 * 2;
      const rsrc_t rs_w = mk_rsrc(wp, role == 0 ? 0 : 64 * stride), rs_o = mk_rsrc(outp, role == 5 ? 4 * DM * 2 : 0);
      asm volatile("s_nop 4\n\tbuffer_load_dword %0, %3, %4, 0 offen nt\n\tbuffer_load_dword %1, %5, %6, 0 offen nt\n\tbuffer_load_dword %2, %7, %8, 0 offen"
                   : "=&v"(tch0), "=&v"(tch1), "=&v"(tch2) : "v"((u32)(lane < 4 ? lane * 4096 : OOB)), "s"(rs_v), "v"((u32)(lane * stride)), "s"(rs_w), "v"((u32)(lane == 0 ? 0 : OOB)), "s"(rs_o) : "memory");
    }
    STAMP(0);
    WAITVM(9);
    asm volatile("" : "+v"(gen), "+v"(xa), "+v"(xb), "+v"(junk));
    const u64 tag64 = (((u64)(u32)__builtin_amdgcn_readfirstlane(gen.y) << 32) | (u32)__builtin_amdgcn_readfirstlane(gen.x)) + 1ull;
    tag_lo = (u32)tag64; tag_hi = (u32)(tag64 >> 32);
    lds_barrier();
    if (w == 7) st8_sc1(v2u{tag_lo, tag_hi}, lane == 0 ? WS_FLAG + c * 8 : OOB, rs_ws);
  }

#pragma unroll 1
  for (int it = 0; it < 2; ++it) {
    {
      STAGE_VARS();
      if (it == 1) {
        if (role == 0) idle<SLP_X_NONE>(); else if (role == 3) idle<SLP_X_S3>(); else if (role == 4) idle<SLP_X_S4>();
        if (w < 4) {
          const u32 base = S_X2 + g * 5120 + w * 64;
          const u32 o1 = base + (lane >> 2) * 256 + (lane & 3) * 16, o2 = lane < 16 ? base + (16 + (lane >> 2)) * 256 + (lane & 3) * 16 : OOB;
          _Pragma("nounroll") for (u32 spin = 0; spin < SPIN_MAX; ++spin) {
            asm volatile("buffer_load_dwordx4 %0, %2, %4, 0 offen sc1\n\tbuffer_load_dwordx4 %1, %3, %4, 0 offen sc1\n\ts_waitcnt vmcnt(0)"
                         : "=&v"(xa), "=&v"(xb) : "v"(o1), "v"(o2), "s"(rs_ws) : "memory");
            if (all_valid(umax3(umax4(xa), umax4(xb), 0u))) break;
          }
        }
      }
      STAMP(1 + it * 12);
      v4u wqf[4];
      if (w >= 3) {
        const u32 wf0 = WF0();
#pragma unroll
        for (int s = 0; s < 2; ++s)
#pragma unroll
          for (int t = 0; t < 2; ++t) wqf[s * 2 + t] = wfrag(L + L_WQ + (w - 3) * 4096 + s * 2048 + (wf0 ^ (t * 32)));
      }
      if (w < 4) {
        float fa[8], fb[8];
#pragma unroll
        for (int j = 0; j < 4; ++j) { fa[2 * j] = bflo(xa[j]); fa[2 * j + 1] = bfhi(xa[j]); fb[2 * j] = bflo(xb[j]); fb[2 * j + 1] = bfhi(xb[j]); }
        *(LAS v4u*)(L + L_XR + w * 1280 + lane * 16) = xa;
        if (lane < 16) *(LAS v4u*)(L + L_XR + w * 1280 + 1024 + lane * 16) = xb;
        ln_row(fa, fb, lane, L + L_XN + w * XN_STRIDE);
      }
      xa = v4u{0, 0, 0, 0}; xb = v4u{0, 0, 0, 0};
      lds_barrier();
      if (w >= 3) {
        f4 acc0 = {0, 0, 0, 0}, acc1 = {0, 0, 0, 0};
#pragma unroll
        for (int s = 0; s < 2; ++s) {
          const v4u xf = *(LAS v4u*)(L + L_XN + (c16 & 3) * XN_STRIDE + ((bh & 1) * 320 + (w - 3) * 64 + s * 32 + 8 * g4) * 2);
          acc0 = mfma(wqf[s * 2], xf, acc0);
          acc1 = mfma(wqf[s * 2 + 1], xf, acc1);
        }
        if (c16 < 4) {
          *(LAS f4*)(L + L_RQ + (w - 3) * 512 + c16 * 64 + g4 * 16) = acc0;
          *(LAS f4*)(L + L_RQ + (w - 3) * 512 + 256 + c16 * 64 + g4 * 16) = acc1;
        }
      }
      lds_barrier();
      if (w == 0) {
        const f2 s = red_sum<5, 1>(L + L_RQ, lane);
        const u32 d = nosent(cvtpk(s.x, s.y));
        if (it == 0) {
          const u32 dn = dppu<SHL1>(d);
          st16_sc1(v4u{d, dn, tag_lo, tag_hi}, (lane & 1) ? OOB : WS_Q1 + c * 512 + (lane >> 1) * 16, rs_ws);
        } else {
          st16_sc1(quad_gather(d), (lane & 3) ? OOB : S_Q2 + (h * 8 + (bh >> 1) * 2 + (bh & 1)) * 256 + lane * 4, rs_ws);
        }
      }
      STAMP(2 + it * 12);
    }
    v2u qv;
    if (it == 0) {
      STAGE_VARS();
      KV_DESC();
      asm volatile("s_nop 4" ::: "memory");
      const u32 q1off = WS_Q1 + (g + 8 * ((h & 3) * 8 + (lane >> 3))) * 512 + b * 128 + (lane & 7) * 16;
      v4u p1, fl0, fl1;
      WAITVM(3); LDK(6); WAITVM(3); LDK(7); WAITVM(3); LDK(8); WAITVM(3); LDK(9);
      asm volatile("" : "+v"(tch0), "+v"(tch1), "+v"(tch2));
      WAITVM(3); LDK(10); WAITVM(3); LDK(11); WAITVM(3); LDK(12); WAITVM(3); LDK(13);
      WAITVM(3); LDK(14); WAITVM(3); LDK(15);
      WAITVM(3); LDV(0); WAITVM(3); LDV(1); WAITVM(3); LDV(2); WAITVM(3); LDV(3);
      asm volatile("buffer_load_dwordx4 %0, %1, %2, 0 offen sc1" : "=v"(p1) : "v"(q1off), "s"(rs_ws) : "memory");
      WAITVM(3); LDV(4); WAITVM(3); LDV(5); WAITVM(3); LDV(6); WAITVM(3); LDV(7);
      asm volatile("buffer_load_dwordx4 %0, %2, %3, 0 offen sc1\n\tbuffer_load_dwordx4 %1, %2, %3, 0 offen offset:1024 sc1"
                   : "=&v"(fl0), "=&v"(fl1) : "v"(w == 7 ? WS_FLAG + lane * 16 : OOB), "s"(rs_ws) : "memory");
      WAITVM(3); LDV(8); WAITVM(3); LDV(9); WAITVM(3); LDV(10); WAITVM(3); LDV(11);
      WAITVM(3); LDV(12); WAITVM(3); LDV(13); WAITVM(3); LDV(14); WAITVM(3); LDV(15);
      if (role == 3) {
        const rsrc_t rs = mk_rsrc(ka->wo + (size_t)(1024 * g + 128 * w) * DM + 32 * r, 128 * 1280);
        const u32 vo = DMA_LANE(1280);
        asm volatile("s_nop 4" ::: "memory");
#pragma unroll
        for (int i = 0; i < 8; ++i) dma16(L_WR + w * 8192 + i * 1024, vo, rs, i * 16 * 1280);
      }
      STAMP(3);
      asm volatile("" : "+v"(p1), "+v"(fl0), "+v"(fl1));
      _Pragma("nounroll") for (u32 spin = 0; spin < SPIN_MAX; ++spin) {
        if (__builtin_amdgcn_ballot_w64(p1.z != tag_lo || p1.w != tag_hi) == 0ull) break;
        asm volatile("buffer_load_dwordx4 %0, %1, %2, 0 offen sc1\n\ts_waitcnt vmcnt(0)" : "=&v"(p1) : "v"(q1off), "s"(rs_ws) : "memory");
      }
      qv = v2u{p1.x, p1.y};
      if (w == 7) {
        _Pragma("nounroll") for (u32 spin = 0; spin < SPIN_MAX; ++spin) {
          const bool bad = fl0.x != tag_lo || fl0.y != tag_hi || fl0.z != tag_lo || fl0.w != tag_hi || fl1.x != tag_lo || fl1.y != tag_hi || fl1.z != tag_lo || fl1.w != tag_hi;
          if (__builtin_amdgcn_ballot_w64(bad) == 0ull) break;
          asm volatile("buffer_load_dwordx4 %0, %2, %3, 0 offen sc1\n\tbuffer_load_dwordx4 %1, %2, %3, 0 offen offset:1024 sc1\n\ts_waitcnt vmcnt(0)"
                       : "=&v"(fl0), "=&v"(fl1) : "v"(WS_FLAG + lane * 16), "s"(rs_ws) : "memory");
        }
      }
    } else {
      STAGE_VARS();
      const u32 off = S_Q2 + ((h * 8 + (lane >> 3)) * 4 + b) * 64 + (lane & 7) * 8;
      _Pragma("nounroll") for (u32 spin = 0; spin < SPIN_MAX; ++spin) {
        asm volatile("buffer_load_dwordx2 %0, %1, %2, 0 offen sc1\n\ts_waitcnt vmcnt(0)" : "=&v"(qv) : "v"(off), "s"(rs_ws) : "memory");
        if (all_valid(qv.x > qv.y ? qv.x : qv.y)) break;
      }
    }
    STAMP(4 + it * 12);
    const u32 pbase = it * PASS_STRIDE;
    {
      STAGE_VARS();
      {
        float q0 = bflo(qv.x), q1 = bfhi(qv.x), q2 = bflo(qv.y), q3 = bfhi(qv.y);
        q0 += dpp<ROR8>(q0); q1 += dpp<ROR8>(q1); q2 += dpp<ROR8>(q2); q3 += dpp<ROR8>(q3);
        *(LAS v2u*)(L + L_QB + w * 256 + g4 * 64 + (lane & 7) * 8) = v2u{cvtpk(q0, q1), cvtpk(q2, q3)};
      }
      asm volatile("s_waitcnt lgkmcnt(0)" ::: "memory");
      const v4u qa0 = *(LAS v4u*)(L + L_QB + w * 256 + (c16 & 1) * 64 + g4 * 16);
      const v4u qa1 = *(LAS v4u*)(L + L_QB + w * 256 + 128 + (c16 & 1) * 64 + g4 * 16);
#pragma unroll
      for (int i = 0; i < 16; ++i) asm volatile("" : "+a"(kr[i]));
      f4 sc[8];
#pragma unroll
      for (int t = 0; t < 8; ++t) {
        sc[t] = mfma(qa0, kr[(t >> 1) * 4 + (t & 1) * 2], f4{0, 0, 0, 0});
        sc[t] = mfma(qa1, kr[(t >> 1) * 4 + (t & 1) * 2 + 1], sc[t]);
      }
      float sa = sc[0][0], sb = sc[1][0];
      sa = sel<0x1, 0xC>(sa, sc[1][1]); sb = sel<0x1, 0xC>(sb, sc[0][1]);
      sa = sel<0x2, 0x3>(sa, sc[2][0]); sb = sel<0x2, 0x3>(sb, sc[3][0]);
      sa = sel<0x2, 0xC>(sa, sc[3][1]); sb = sel<0x2, 0xC>(sb, sc[2][1]);
      sa = sel<0x4, 0x3>(sa, sc[4][0]); sb = sel<0x4, 0x3>(sb, sc[5][0]);
      sa = sel<0x4, 0xC>(sa, sc[5][1]); sb = sel<0x4, 0xC>(sb, sc[4][1]);
      sa = sel<0x8, 0x3>(sa, sc[6][0]); sb = sel<0x8, 0x3>(sb, sc[7][0]);
      sa = sel<0x8, 0xC>(sa, sc[7][1]); sb = sel<0x8, 0xC>(sb, sc[6][1]);
      const float s2 = (sa + dpp<ROR8>(sb)) * (0.08838834764831845f * 1.4426950408889634f);
      const float mw = wave_max(s2);
      const float pe = __builtin_amdgcn_exp2f(s2 - mw);
      const float lw = wave_sum(pe);
      const u32 pp = cvtpk(pe, dpp<SHL1>(pe));
      float oc[8] = {0, 0, 0, 0, 0, 0, 0, 0};
#define VPAIR(m) do { _Pragma("unroll") for (int u = 0; u < 4; ++u) { u32 t_; \
      asm volatile("v_perm_b32 %2, %1, %0, %3\n\tv_perm_b32 %1, %1, %0, %4\n\tv_mov_b32 %0, %2" : "+v"(vr[2 * (m)][u]), "+v"(vr[2 * (m) + 1][u]), "=&v"(t_) : "s"(0x05040100u), "s"(0x07060302u)); } } while (0)
#define PV(m, bc) asm volatile("s_nop 1\n\t" \
      "v_dot2c_f32_bf16_dpp %0, %8, %9 row_newbcast:" #bc " row_mask:0xf bank_mask:0xf\n\t" \
      "v_dot2c_f32_bf16_dpp %1, %8, %13 row_newbcast:" #bc " row_mask:0xf bank_mask:0xf\n\t" \
      "v_dot2c_f32_bf16_dpp %2, %8, %10 row_newbcast:" #bc " row_mask:0xf bank_mask:0xf\n\t" \
      "v_dot2c_f32_bf16_dpp %3, %8, %14 row_newbcast:" #bc " row_mask:0xf bank_mask:0xf\n\t" \
      "v_dot2c_f32_bf16_dpp %4, %8, %11 row_newbcast:" #bc " row_mask:0xf bank_mask:0xf\n\t" \
      "v_dot2c_f32_bf16_dpp %5, %8, %15 row_newbcast:" #bc " row_mask:0xf bank_mask:0xf\n\t" \
      "v_dot2c_f32_bf16_dpp %6, %8, %12 row_newbcast:" #bc " row_mask:0xf bank_mask:0xf\n\t" \
      "v_dot2c_f32_bf16_dpp %7, %8, %16 row_newbcast:" #bc " row_mask:0xf bank_mask:0xf\n\ts_nop 2" \
      : "+v"(oc[0]), "+v"(oc[1]), "+v"(oc[2]), "+v"(oc[3]), "+v"(oc[4]), "+v"(oc[5]), "+v"(oc[6]), "+v"(oc[7]) \
      : "v"(pp), "v"(vr[2 * (m)][0]), "v"(vr[2 * (m)][1]), "v"(vr[2 * (m)][2]), "v"(vr[2 * (m)][3]), \
        "v"(vr[2 * (m) + 1][0]), "v"(vr[2 * (m) + 1][1]), "v"(vr[2 * (m) + 1][2]), "v"(vr[2 * (m) + 1][3]))
      if (it == 0) {
#pragma unroll
        for (int i = 0; i < 12; ++i) asm volatile("" : "+v"(vr[i]));
        VPAIR(0); VPAIR(1); VPAIR(2); VPAIR(3); VPAIR(4); VPAIR(5);
      }
      PV(0, 0); PV(1, 2); PV(2, 4); PV(3, 6); PV(4, 8); PV(5, 10);
      if (it == 0) {
        if (role == 3) WAITVM(10); else WAITVM(2);
        asm volatile("" : "+v"(vr[12]), "+v"(vr[13]));
        VPAIR(6);
      }
      PV(6, 12);
      if (it == 0) {
        if (role == 3) WAITVM(8); else WAITVM(0);
        asm volatile("" : "+v"(vr[14]), "+v"(vr[15]));
        VPAIR(7);
      }
      PV(7, 14);
      STAMP(5 + it * 12);
      float t4[4], u2[2];
#pragma unroll
      for (int d = 0; d < 4; ++d) {
        auto s = __builtin_amdgcn_permlane32_swap(__float_as_uint(oc[d]), __float_as_uint(oc[d + 4]), false, false);
        t4[d] = __uint_as_float(s[0]) + __uint_as_float(s[1]);
      }
#pragma unroll
      for (int d = 0; d < 2; ++d) {
        auto s = __builtin_amdgcn_permlane16_swap(__float_as_uint(t4[d]), __float_as_uint(t4[d + 2]), false, false);
        u2[d] = __uint_as_float(s[0]) + __uint_as_float(s[1]);
      }
      *(LAS f2*)(L + L_RA + w * 512 + c16 * 32 + g4 * 8) = f2{u2[0], u2[1]};
      if (lane == 0) *(LAS f2*)(L + L_RA + 4096 + w * 8) = f2{mw, lw};
      lds_barrier();
      if (w == 0) {
        f2 st[8];
#pragma unroll
        for (int i = 0; i < 8; ++i) st[i] = *(LAS f2*)(L + L_RA + 4096 + i * 8);
        float M = st[0].x;
#pragma unroll
        for (int i = 1; i < 8; ++i) M = fmaxf(M, st[i].x);
        float Lsum = 0.f, o0 = 0.f, o1 = 0.f;
#pragma unroll
        for (int i = 0; i < 8; ++i) {
          const float f = __builtin_amdgcn_exp2f(st[i].x - M);
          const f2 v = *(LAS f2*)(L + L_RA + i * 512 + lane * 8);
          Lsum = fmaf(f, st[i].y, Lsum); o0 = fmaf(f, v.x, o0); o1 = fmaf(f, v.y, o1);
        }
        const float inv = __builtin_amdgcn_rcpf(Lsum);
        const u32 d = nosent(cvtpk(o0 * inv, o1 * inv));
        st16_sc1(quad_gather(d), (lane & 3) ? OOB : S_O0 + pbase + ((h * 2 + half) * 4 + b) * 256 + lane * 4, rs_ws);
        st8_sc1(v2u{nosent(__float_as_uint(M)), nosent(__float_as_uint(Lsum))}, lane == 0 ? S_OST0 + pbase + (h * 4 + b) * 16 + half * 8 : OOB, rs_ws);
      }
      STAMP(6 + it * 12);
    }
    if (role == 3) {
      STAGE_VARS();
      const int hh = 8 * g + w;
      const u32 o0off = S_O0 + pbase + (hh * 8 + (c16 & 3)) * 256 + (c16 >> 2) * 64 + g4 * 16;
      const u32 stoff = S_OST0 + pbase + (hh * 4 + (c16 & 3)) * 16;
      v4u oa, ob, st;
      asm volatile("buffer_load_dwordx4 %0, %3, %5, 0 offen sc1\n\tbuffer_load_dwordx4 %1, %3, %5, 0 offen offset:1024 sc1\n\tbuffer_load_dwordx4 %2, %4, %5, 0 offen sc1\n\ts_waitcnt vmcnt(3)"
                   : "=&v"(oa), "=&v"(ob), "=&v"(st) : "v"(o0off), "v"(stoff), "s"(rs_ws) : "memory");
      v4u wf[8];
      const u32 wf0 = WF0();
#pragma unroll
      for (int s = 0; s < 4; ++s)
#pragma unroll
        for (int t = 0; t < 2; ++t) wf[s * 2 + t] = wfrag(L + L_WR + w * 8192 + s * 2048 + (wf0 ^ (t * 32)));
      asm volatile("s_waitcnt vmcnt(0)" : "+v"(oa), "+v"(ob), "+v"(st)::"memory");
      _Pragma("nounroll") for (u32 spin = 0; spin < SPIN_MAX; ++spin) {
        if (all_valid(umax3(umax4(oa), umax4(ob), umax4(st)))) break;
        asm volatile("buffer_load_dwordx4 %0, %3, %5, 0 offen sc1\n\tbuffer_load_dwordx4 %1, %3, %5, 0 offen offset:1024 sc1\n\tbuffer_load_dwordx4 %2, %4, %5, 0 offen sc1\n\ts_waitcnt vmcnt(0)"
                     : "=&v"(oa), "=&v"(ob), "=&v"(st) : "v"(o0off), "v"(stoff), "s"(rs_ws) : "memory");
      }
      STAMP(7 + it * 12);
      const float m0 = __uint_as_float(st.x), l0 = __uint_as_float(st.y), m1 = __uint_as_float(st.z), l1 = __uint_as_float(st.w);
      const float mx = fmaxf(m0, m1);
      const float w0 = l0 * __builtin_amdgcn_exp2f(m0 - mx), w1 = l1 * __builtin_amdgcn_exp2f(m1 - mx);
      const float inv = __builtin_amdgcn_rcpf(w0 + w1), a0 = w0 * inv, a1 = w1 * inv;
      v4u of;
#pragma unroll
      for (int j = 0; j < 4; ++j) of[j] = cvtpk(fmaf(a0, bflo(oa[j]), a1 * bflo(ob[j])), fmaf(a0, bfhi(oa[j]), a1 * bfhi(ob[j])));
      f4 acc[8];
#pragma unroll
      for (int i = 0; i < 8; ++i) acc[i] = mfma(wf[i], of, f4{0, 0, 0, 0});
#pragma unroll
      for (int t = 0; t < 2; ++t) {
        f4 y;
#pragma unroll
        for (int j = 0; j < 4; ++j) y[j] = acc[t][j] + dpp<SHL4>(acc[2 + t][j]) + dpp<SHL8>(acc[4 + t][j]) + dpp<SHL12>(acc[6 + t][j]);
        if (c16 < 4) *(LAS f4*)(L + L_RR + w * 512 + t * 256 + c16 * 64 + g4 * 16) = y;
      }
      lds_barrier();
      if (w == 0) {
        f2 s = red_sum<8, 1>(L + L_RR, lane);
        if (g == 0) { const u32 x = *(LAS u32*)(L + L_XR + g4 * 1280 + (32 * r + 2 * c16) * 2); s.x += bflo(x); s.y += bfhi(x); }
        const u32 d = nosent(cvtpk(s.x, s.y));
        st16_sc1(quad_gather(d), (lane & 3) < 2 ? S_X1P0 + pbase + (lane & 3) * 20480 + (g * 20 + r) * 256 + (lane >> 2) * 16 : OOB, rs_ws);
      }
    } else if (role == 4) {
      STAGE_VARS();
      const int j4 = (g - 4) * 27 + r;
      if (it == 0) {
        lds_barrier();
        const rsrc_t rs = mk_rsrc(((w & 1) ? ka->wu : ka->wg) + (size_t)(160 * (w >> 1)) * FF + 32 * j4, 160 * 3456);
        const u32 vo = DMA_LANE(3456);
        asm volatile("s_nop 4" ::: "memory");
#pragma unroll
        for (int i = 0; i < 10; ++i) dma16(L_WR + w * 10240 + i * 1024, vo, rs, i * 16 * 3456);
        WAITVM(0);
      } else {
        idle<SLP_S4>();
      }
      v4u wf[10];
      const u32 wf0 = WF0();
#pragma unroll
      for (int s = 0; s < 5; ++s)
#pragma unroll
        for (int t = 0; t < 2; ++t) wf[s * 2 + t] = wfrag(L + L_WR + w * 10240 + s * 2048 + (wf0 ^ (t * 32)));
#pragma unroll
      for (int i = 0; i < 10; ++i) asm volatile("" : "+a"(wf[i]));
      if (w < 4) {
        const u32 base = S_X1P0 + pbase + (g - 4) * 20480 + w * 64;
        const u32 o1 = base + (lane >> 2) * 256 + (lane & 3) * 16, o2 = lane < 16 ? base + (16 + (lane >> 2)) * 256 + (lane & 3) * 16 : OOB;
        v4u pa[4], pb[4];
        _Pragma("nounroll") for (u32 spin = 0; spin < SPIN_MAX; ++spin) {
          asm volatile("buffer_load_dwordx4 %0, %8, %10, 0 offen sc1\n\tbuffer_load_dwordx4 %1, %9, %10, 0 offen sc1\n\t"
                       "buffer_load_dwordx4 %2, %8, %10, %11 offen sc1\n\tbuffer_load_dwordx4 %3, %9, %10, %11 offen sc1\n\t"
                       "buffer_load_dwordx4 %4, %8, %10, %12 offen sc1\n\tbuffer_load_dwordx4 %5, %9, %10, %12 offen sc1\n\t"
                       "buffer_load_dwordx4 %6, %8, %10, %13 offen sc1\n\tbuffer_load_dwordx4 %7, %9, %10, %13 offen sc1\n\ts_waitcnt vmcnt(0)"
                       : "=&v"(pa[0]), "=&v"(pb[0]), "=&v"(pa[1]), "=&v"(pb[1]), "=&v"(pa[2]), "=&v"(pb[2]), "=&v"(pa[3]), "=&v"(pb[3])
                       : "v"(o1), "v"(o2), "s"(rs_ws), "s"(5120u), "s"(10240u), "s"(15360u) : "memory");
          u32 mx = 0;
#pragma unroll
          for (int q = 0; q < 4; ++q) mx = umax3(mx, umax4(pa[q]), umax4(pb[q]));
          if (all_valid(mx)) break;
        }
        STAMP(8 + it * 12);
        float fa[8], fb[8];
#pragma unroll
        for (int j = 0; j < 4; ++j) {
          fa[2 * j] = (bflo(pa[0][j]) + bflo(pa[1][j])) + (bflo(pa[2][j]) + bflo(pa[3][j]));
          fa[2 * j + 1] = (bfhi(pa[0][j]) + bfhi(pa[1][j])) + (bfhi(pa[2][j]) + bfhi(pa[3][j]));
          fb[2 * j] = (bflo(pb[0][j]) + bflo(pb[1][j])) + (bflo(pb[2][j]) + bflo(pb[3][j]));
          fb[2 * j + 1] = (bfhi(pb[0][j]) + bfhi(pb[1][j])) + (bfhi(pb[2][j]) + bfhi(pb[3][j]));
        }
        ln_row(fa, fb, lane, L + L_XN + w * XN_STRIDE);
      }
      lds_barrier();
      f4 acc0 = {0, 0, 0, 0}, acc1 = {0, 0, 0, 0};
#pragma unroll
      for (int s = 0; s < 5; ++s) {
        const v4u xf = *(LAS v4u*)(L + L_XN + (c16 & 3) * XN_STRIDE + ((w >> 1) * 160 + s * 32 + 8 * g4) * 2);
        acc0 = mfma(wf[s * 2], xf, acc0);
        acc1 = mfma(wf[s * 2 + 1], xf, acc1);
      }
      if (c16 < 4) {
        *(LAS f4*)(L + L_RR + w * 512 + c16 * 64 + g4 * 16) = acc0;
        *(LAS f4*)(L + L_RR + w * 512 + 256 + c16 * 64 + g4 * 16) = acc1;
      }
      lds_barrier();
      if (w == 0) {
        const f2 ga = red_sum<4, 2>(L + L_RR, lane), up = red_sum<4, 2>(L + L_RR + 512, lane);
        const u32 d = nosent(cvtpk((ga.x > 0.f ? ga.x : 0.f * ga.x) * up.x, (ga.y > 0.f ? ga.y : 0.f * ga.y) * up.y));
        st16_sc1(quad_gather(d), (lane & 3) ? OOB : S_HB0 + pbase + j4 * 256 + lane * 4, rs_ws);
      }
    } else if (role == 5) {
      STAGE_VARS();
      const int s0 = w < 6 ? 7 * w : 42 + 6 * (w - 6);
      if (it == 0) {
        lds_barrier();
        const rsrc_t rs = mk_rsrc(ka->wd + (size_t)(32 * s0) * DM + 32 * r, (w < 6 ? 224 : 192) * 1280);
        const u32 vo = DMA_LANE(1280);
        asm volatile("s_nop 4" ::: "memory");
#pragma unroll
        for (int i = 0; i < 12; ++i) dma16(L_WR + s0 * 2048 + i * 1024, vo, rs, i * 16 * 1280);
        if (w < 6) { dma16(L_WR + s0 * 2048 + 12 * 1024, vo, rs, 12 * 16 * 1280); dma16(L_WR + s0 * 2048 + 13 * 1024, vo, rs, 13 * 16 * 1280); }
        WAITVM(0);
      }
      idle<SLP_S5>();
      v4u wf[14];
      const u32 wf0 = WF0();
#pragma unroll
      for (int s = 0; s < 6; ++s)
#pragma unroll
        for (int t = 0; t < 2; ++t) wf[s * 2 + t] = wfrag(L + L_WR + (s0 + s) * 2048 + (wf0 ^ (t * 32)));
      {
        const int s6 = w < 6 ? s0 + 6 : s0;
        wf[12] = wfrag(L + L_WR + s6 * 2048 + wf0); wf[13] = wfrag(L + L_WR + s6 * 2048 + (wf0 ^ 32));
      }
#pragma unroll
      for (int i = 0; i < 14; ++i) asm volatile("" : "+a"(wf[i]));
      const int slot = c16 >> 2;
      const u32 hoff = S_HB0 + pbase + (c16 & 3) * 64 + g4 * 16;
      const u32 oA = hoff + (s0 + slot) * 256;
      const u32 oB = (slot < (w < 6 ? 3 : 2)) ? hoff + (s0 + 4 + slot) * 256 : OOB;
      v4u ha, hb;
      u32 rx0, rx1, rx2, rx3;
      const u32 rxo = w == 7 ? S_X1P0 + pbase + r * 256 + lane * 4 : OOB;
      _Pragma("nounroll") for (u32 spin = 0; spin < SPIN_MAX; ++spin) {
        asm volatile("buffer_load_dwordx4 %0, %6, %9, 0 offen sc1\n\tbuffer_load_dwordx4 %1, %7, %9, 0 offen sc1\n\t"
                     "buffer_load_dword %2, %8, %9, 0 offen sc1\n\tbuffer_load_dword %3, %8, %9, %10 offen sc1\n\tbuffer_load_dword %4, %8, %9, %11 offen sc1\n\tbuffer_load_dword %5, %8, %9, %12 offen sc1\n\ts_waitcnt vmcnt(0)"
                     : "=&v"(ha), "=&v"(hb), "=&v"(rx0), "=&v"(rx1), "=&v"(rx2), "=&v"(rx3)
                     : "v"(oA), "v"(oB), "v"(rxo), "s"(rs_ws), "s"(5120u), "s"(10240u), "s"(15360u) : "memory");
        if (all_valid(umax3(umax3(umax4(ha), umax4(hb), rx0), umax3(rx1, rx2, rx3), 0u))) break;
      }
      STAMP(9 + it * 12);
      f4 acc[8];
#pragma unroll
      for (int i = 0; i < 8; ++i) acc[i] = mfma(wf[i], ha, f4{0, 0, 0, 0});
#pragma unroll
      for (int i = 0; i < 4; ++i) acc[i] = mfma(wf[8 + i], hb, acc[i]);
      if (w < 6) { acc[4] = mfma(wf[12], hb, acc[4]); acc[5] = mfma(wf[13], hb, acc[5]); }
#pragma unroll
      for (int t = 0; t < 2; ++t) {
        f4 y;
#pragma unroll
        for (int j = 0; j < 4; ++j) y[j] = acc[t][j] + dpp<SHL4>(acc[2 + t][j]) + dpp<SHL8>(acc[4 + t][j]) + dpp<SHL12>(acc[6 + t][j]);
        if (c16 < 4) *(LAS f4*)(L + L_RR + w * 512 + t * 256 + c16 * 64 + g4 * 16) = y;
      }
      if (w == 7)
        *(LAS f2*)(L + L_RR + 8 * 512 + ((lane >> 3) & 1) * 256 + g4 * 64 + (lane & 7) * 8) =
            f2{(bflo(rx0) + bflo(rx1)) + (bflo(rx2) + bflo(rx3)), (bfhi(rx0) + bfhi(rx1)) + (bfhi(rx2) + bfhi(rx3))};
      lds_barrier();
      if (w == 0) {
        const f2 s = red_sum<9, 1>(L + L_RR, lane);
        const u32 d0 = cvtpk(s.x, s.y);
        if (it == 0) {
          const v4u q = quad_gather(nosent(d0));
          const u32 off = S_X2 + (lane & 3) * 5120 + r * 256 + (lane >> 2) * 16;
          st16_sc1(q, off, rs_ws);
          st16_sc1(q, off + 4 * 5120, rs_ws);
        } else {
          const v4u q = quad_gather(d0);
          const rsrc_t rs_o = mk_rsrc(outp, 4 * DM * 2);
          asm volatile("s_nop 4\n\tbuffer_store_dwordx4 %0, %1, %2, 0 offen\n\ts_nop 1" ::"v"(q), "v"((lane & 3) ? OOB : (u32)(g4 * 1280 + r * 64 + (c16 >> 2) * 16)), "s"(rs_o) : "memory");
          if (r == 0) st8_sc1(v2u{tag_lo, tag_hi}, lane == 0 ? WS_G : OOB, rs_ws);
        }
      }
    }
    STAMP(10 + it * 12);
  }
  STAMP_OUT();
  asm volatile(".Lfd_code_end:");
}

}

extern "C" void kernel_launch(void* const* d_in, const int* in_sizes, int n_in, void* d_out, int out_size, void* d_ws,
                              size_t ws_size, hipStream_t stream) {
  Args a;
  a.x = (const u16*)d_in[0];
  a.k = (const u16*)d_in[1];
  a.v = (const u16*)d_in[2];
  a.wq = (const u16*)d_in[3];
  a.wo = (const u16*)d_in[4];
  a.wg = (const u16*)d_in[6];
  a.wu = (const u16*)d_in[7];
  a.wd = (const u16*)d_in[8];
  a.out = (u16*)d_out;
  a.ws = (unsigned char*)d_ws;
  (void)hipFuncSetAttribute((const void*)fused_decode, hipFuncAttributeMaxDynamicSharedMemorySize, (int)L_END);
  hipLaunchKernelGGL(fused_decode, dim3(NWG), dim3(NT), L_END, stream, a);
}
```

```cpp
#include <hip/hip_runtime.h>
#include <hip/hip_bf16.h>

typedef unsigned int u32;
typedef unsigned long long u64;
typedef unsigned short u16;
typedef u32 v4u __attribute__((ext_vector_type(4)));
typedef u32 v2u __attribute__((ext_vector_type(2)));
typedef float f4 __attribute__((ext_vector_type(4)));
typedef float f2 __attribute__((ext_vector_type(2)));
typedef __bf16 bf16x8 __attribute__((ext_vector_type(8)));
typedef short s16x4 __attribute__((ext_vector_type(4)));
#define LAS __attribute__((address_space(3)))

namespace {

constexpr int NWG = 256, NT = 512;
constexpr int DM = 640, SK = 4096, WIN = 1024, DH = 128, FF = 1728;

constexpr u32 WS_G = 0;
constexpr u32 WS_FLAG = 4096;
constexpr u32 WS_Q1 = 8192;
constexpr u32 WS_S = 262144;
constexpr u32 O_SLOT = 384;
constexpr u32 S_O0 = WS_S, S_X1P0 = S_O0 + 256 * O_SLOT, S_HB0 = S_X1P0 + 40960, S_X2 = S_HB0 + 13824;
constexpr u32 S_O1 = S_X2 + 40960;
constexpr u32 PASS_STRIDE = S_O1 - S_O0;
constexpr u32 S_Q2 = S_O1 + (S_X2 - S_O0);
constexpr u32 WS_END = WS_S + 256 * 2048;
static_assert(S_Q2 + 65536 <= WS_END, "sentinel region too small");
static_assert((S_X1P0 % 128) == 0 && (S_X2 % 128) == 0 && (S_O1 % 128) == 0 && (S_Q2 % 128) == 0 && (S_HB0 % 128) == 0, "alignment");
constexpr u32 WS_BYTES = 4u << 20;
constexpr u32 OOB = 0x80000000u;

constexpr u32 L_WR = 0;
constexpr u32 L_WQ = 110592;
constexpr u32 L_XN = 131072;
constexpr u32 XN_STRIDE = 1344;
constexpr u32 L_XR = L_XN + 4 * XN_STRIDE;
constexpr u32 L_RA = L_XR + 5120;
constexpr u32 L_RR = L_RA + 4096 + 128;
constexpr u32 L_RQ = L_RR + 9 * 512;
constexpr u32 L_CP = L_RQ + 4 * 512;
constexpr u32 L_CS = L_CP + 8 * 128;
constexpr u32 L_ST = L_CS + 3 * 128;
constexpr u32 L_QB = L_ST + 128;
constexpr u32 L_END = L_QB + 2048;
static_assert(L_END <= 160 * 1024 && L_END > 80 * 1024, "LDS");

constexpr u32 SENT = 0xFFFFFFFFu;
constexpr u32 SPIN_MAX = 1u << 16;

#define POLL_SLEEP 0
#define SLP_S4 20
#define SLP_S5 50
#define SLP_X_NONE 90
#define SLP_X_S3 55
#define SLP_X_S4 20

struct Args {
  const u16* x; const u16* k; const u16* v; const u16* wq; const u16* wo;
  const u16* wg; const u16* wu; const u16* wd; u16* out; unsigned char* ws;
};

typedef __amdgpu_buffer_rsrc_t rsrc_t;
__device__ __forceinline__ rsrc_t mk_rsrc(const void* p, u32 bytes) { return __builtin_amdgcn_make_buffer_rsrc((void*)p, 0, (int)bytes, 0x00020000); }

__device__ __forceinline__ float bflo(u32 d) { return __builtin_bit_cast(float, d << 16); }
__device__ __forceinline__ float bfhi(u32 d) { return __builtin_bit_cast(float, d & 0xffff0000u); }
__device__ __forceinline__ u32 cvtpk(float lo, float hi) {
  typedef __bf16 bf16x2 __attribute__((ext_vector_type(2)));
  typedef float f32x2 __attribute__((ext_vector_type(2)));
  f32x2 v = {lo, hi};
  return __builtin_bit_cast(u32, __builtin_convertvector(v, bf16x2));
}
__device__ __forceinline__ u32 nosent(u32 d) { return d < 0xFFFFFFFEu ? d : 0xFFFFFFFEu; }
template <int CTRL> __device__ __forceinline__ float dpp(float x) {
  return __builtin_bit_cast(float, __builtin_amdgcn_mov_dpp(__builtin_bit_cast(int, x), CTRL, 0xf, 0xf, true));
}
template <int CTRL> __device__ __forceinline__ u32 dppu(u32 x) { return (u32)__builtin_amdgcn_mov_dpp((int)x, CTRL, 0xf, 0xf, true); }
template <int RM, int BM> __device__ __forceinline__ float sel(float old, float src) {
  return __builtin_bit_cast(float, __builtin_amdgcn_update_dpp(__builtin_bit_cast(int, old), __builtin_bit_cast(int, src), 0xE4, RM, BM, false));
}
constexpr int ROR8 = 0x128, ROR4 = 0x124, ROR2 = 0x122, ROR1 = 0x121, SHL1 = 0x101, SHL4 = 0x104, SHL8 = 0x108, SHL12 = 0x10C;
__device__ __forceinline__ float wave_sum(float x) {
  x += dpp<ROR8>(x); x += dpp<ROR4>(x); x += dpp<ROR2>(x); x += dpp<ROR1>(x);
  auto s = __builtin_amdgcn_permlane16_swap(__float_as_uint(x), __float_as_uint(x), false, false);
  x = __uint_as_float(s[0]) + __uint_as_float(s[1]);
  auto t = __builtin_amdgcn_permlane32_swap(__float_as_uint(x), __float_as_uint(x), false, false);
  return __uint_as_float(t[0]) + __uint_as_float(t[1]);
}
__device__ __forceinline__ float wave_max(float x) {
  x = fmaxf(x, dpp<ROR8>(x)); x = fmaxf(x, dpp<ROR4>(x)); x = fmaxf(x, dpp<ROR2>(x)); x = fmaxf(x, dpp<ROR1>(x));
  auto s = __builtin_amdgcn_permlane16_swap(__float_as_uint(x), __float_as_uint(x), false, false);
  x = fmaxf(__uint_as_float(s[0]), __uint_as_float(s[1]));
  auto t = __builtin_amdgcn_permlane32_swap(__float_as_uint(x), __float_as_uint(x), false, false);
  return fmaxf(__uint_as_float(t[0]), __uint_as_float(t[1]));
}
__device__ __forceinline__ f4 mfma(v4u a, v4u b, f4 c) {
  return __builtin_amdgcn_mfma_f32_16x16x32_bf16(__builtin_bit_cast(bf16x8, a), __builtin_bit_cast(bf16x8, b), c, 0, 0, 0);
}
__device__ __forceinline__ u32 umax3(u32 a, u32 b, u32 c) { u32 m = a > b ? a : b; return m > c ? m : c; }
__device__ __forceinline__ u32 umax4(v4u v) { return umax3(v.x, v.y, v.z > v.w ? v.z : v.w); }
__device__ __forceinline__ bool all_valid(u32 mx) { return __builtin_amdgcn_ballot_w64(mx == SENT) == 0ull; }
__device__ __forceinline__ void lds_barrier() {
  asm volatile("s_waitcnt lgkmcnt(0)" ::: "memory");
  __builtin_amdgcn_s_barrier();
  asm volatile("" ::: "memory");
}
__device__ __forceinline__ v4u wfrag(LAS unsigned char* p) {
  typedef short v4i16_t __attribute__((ext_vector_type(4)));
  v4i16_t lo = __builtin_amdgcn_ds_read_tr16_b64_v4i16((LAS v4i16_t*)p);
  v4i16_t hi = __builtin_amdgcn_ds_read_tr16_b64_v4i16((LAS v4i16_t*)(p + 256));
  v2u a = __builtin_bit_cast(v2u, lo), b = __builtin_bit_cast(v2u, hi);
  return v4u{a.x, a.y, b.x, b.y};
}
#define WAITVM(n) asm volatile("s_waitcnt vmcnt(" #n ")" ::: "memory")
#define WAITVM_(n) WAITVM(n)
#define WAITVM_I(n) asm volatile("s_waitcnt vmcnt(%0)" ::"n"(n) : "memory")
__device__ __forceinline__ void dma16(u32 ldsaddr, u32 voff, rsrc_t rs, u32 soff) {
  asm volatile("s_mov_b32 m0, %0\n\ts_nop 0\n\tbuffer_load_dwordx4 %1, %2, %3 offen nt lds" ::"s"(ldsaddr), "v"(voff), "s"(rs), "s"(soff) : "memory");
}
__device__ __forceinline__ void st16_sc1(v4u d, u32 voff, rsrc_t rs) {
  asm volatile("buffer_store_dwordx4 %0, %1, %2, 0 offen sc1\n\ts_nop 1" ::"v"(d), "v"(voff), "s"(rs) : "memory");
}
__device__ __forceinline__ void st8_sc1(v2u d, u32 voff, rsrc_t rs) {
  asm volatile("buffer_store_dwordx2 %0, %1, %2, 0 offen sc1\n\ts_nop 1" ::"v"(d), "v"(voff), "s"(rs) : "memory");
}
__device__ __forceinline__ v4u quad_gather(u32 d) {
  return v4u{dppu<0x00>(d), dppu<0x55>(d), dppu<0xAA>(d), dppu<0xFF>(d)};
}
template <int N> __device__ __forceinline__ void idle() {
  if (N > 0) __builtin_amdgcn_s_sleep(N > 127 ? 127 : N);
}

#define LAUNDER_V(x) asm volatile("" : "+v"(x))
#define LAUNDER_S(x) asm volatile("" : "+s"(x))
typedef const __attribute__((address_space(4))) Args* kargs_t;
#define STAGE_VARS() int lane = lane_; LAUNDER_V(lane); const int g4 = lane >> 4, c16 = lane & 15; (void)g4; (void)c16; \
    int w = w_, c = c_; LAUNDER_S(w); LAUNDER_S(c); const int g = c & 7, r = c >> 3; (void)g; (void)r; \
    const int h = 4 * g + (r >> 3), bh = r & 7, b = bh >> 1, half = bh & 1; (void)h; (void)b; (void)half; \
    kargs_t ka = ka_; LAUNDER_S(ka); (void)ka

#define STAMP(i) do { } while (0)
#define STAMP1(i) do { } while (0)
#define STAMPA(i) do { } while (0)
#define STAMPB(i) do { } while (0)
#define STAMPVAL(i, val) do { } while (0)
#define CST(i) do { } while (0)
#define STAMP_OUT() do { } while (0)

constexpr u32 ONES = 0x3F803F80u;
__device__ __forceinline__ float dot2(u32 a, u32 b, float c) {
  typedef __bf16 bf16x2 __attribute__((ext_vector_type(2)));
  return __builtin_amdgcn_fdot2_f32_bf16(__builtin_bit_cast(bf16x2, a), __builtin_bit_cast(bf16x2, b), c, false);
}
__device__ __forceinline__ f2 row_stats(v4u xa, v4u xb) {
  float s1 = 0.f, s2 = 0.f, t1 = 0.f, t2 = 0.f;
#pragma unroll
  for (int j = 0; j < 4; ++j) { s1 = dot2(xa[j], ONES, s1); s2 = dot2(xa[j], xa[j], s2); t1 = dot2(xb[j], ONES, t1); t2 = dot2(xb[j], xb[j], t2); }
  const float mean = wave_sum(s1 + t1) * (1.f / DM);
  const float var = fmaxf(fmaf(-mean, mean, wave_sum(s2 + t2) * (1.f / DM)), 0.f);
  return f2{mean, __builtin_amdgcn_rsqf(var + 1e-5f)};
}
__device__ __forceinline__ u32 red_lane(int lane) { return ((lane >> 3) & 1) * 256 + (lane >> 4) * 64 + (lane & 7) * 8; }
template <int NP, int STRIDE> __device__ __forceinline__ f2 red_sum(LAS unsigned char* p) {
  f2 v[NP];
#pragma unroll
  for (int i = 0; i < NP; ++i) v[i] = *(LAS f2*)(p + i * STRIDE);
#pragma unroll
  for (int st = 1; st < NP; st *= 2)
#pragma unroll
    for (int i = 0; i + st < NP; i += 2 * st) v[i] += v[i + st];
  return v[0];
}
#define MF(d, a, b, c) "v_mfma_f32_16x16x32_bf16 " d ", " a ", " b ", " c "\n\t"
#define MV1(d, s, n) "v_mov_b32_dpp " d ", " s " row_shl:" n " row_mask:0xf bank_mask:0xf bound_ctrl:1\n\t"
#define MVT(s0, s1, s2, s3, n) MV1("v120", s0, n) MV1("v121", s1, n) MV1("v122", s2, n) MV1("v123", s3, n)
#define MVU(s0, s1, s2, s3, n) MV1("v124", s0, n) MV1("v125", s1, n) MV1("v126", s2, n) MV1("v127", s3, n)
#define RT "v[120:123]"
#define RU "v[124:127]"
#define TU_CLOBBER "v120", "v121", "v122", "v123", "v124", "v125", "v126", "v127"

__global__ void __launch_bounds__(NT, 2) fused_decode(Args a_) {
  extern __shared__ __attribute__((aligned(16))) unsigned char smem_[];
  LAS unsigned char* const L = (LAS unsigned char*)smem_;
  const int lane_ = threadIdx.x & 63;
  const int w_ = __builtin_amdgcn_readfirstlane(threadIdx.x >> 6);
  const int c_ = blockIdx.x;
  const kargs_t ka_ = (kargs_t)__builtin_amdgcn_kernarg_segment_ptr();
  const int role = ((c_ & 7) < 4 && (c_ >> 3) < 20) ? 3 : (((c_ & 7) == 4 || (c_ & 7) == 5) && (c_ >> 3) < 27) ? 4 : ((c_ & 7) == 6 && (c_ >> 3) < 20) ? 5 : 0;
  const rsrc_t rs_ws = mk_rsrc(a_.ws, WS_BYTES);
  u16* const outp = a_.out;
  STAMP(11);

  v4u kr[16], vr[16];
  v4u xa, xb;
  u32 tag_lo, tag_hi;
  rsrc_t rs_wr;
  u32 wr_lane, wr_step, wr_lds;
#define ROLE_DMA(i) dma16(wr_lds + (i) * 1024, wr_lane, rs_wr, (i) * wr_step)
#define NE3 6
#define NE 4
#define EARLY_DMA(i) do { if (it == 0 && ((i) < NE ? role != 0 : (i) < NE3 ? role == 3 : false)) ROLE_DMA(i); } while (0)
#define WAIT_EARLY(base, n) do { if (role == 0) WAITVM_I(base); else if (role == 3) WAITVM_I((base) + (NE3 < (n) ? NE3 : (n))); else WAITVM_I((base) + (NE < (n) ? NE : (n))); } while (0)
#define KV_DESC() const size_t kvoff = ((size_t)(b * 32 + h) * SK + (SK - WIN) + half * 512 + w * 64) * DH; \
  const rsrc_t rs_k = mk_rsrc(ka->k + kvoff, 16384), rs_v = mk_rsrc(ka->v + kvoff, 16384); (void)rs_v; \
  const u32 voK0 = (lane & 7) * 1024 + ((lane >> 3) & 1) * 64 + g4 * 16, voK1 = voK0 + 8192, voV = lane * 16; (void)voV
#define LDK(i) asm volatile("buffer_load_dwordx4 %0, %1, %2, 0 offen offset:%3 nt" : "+a"(kr[i]) : "v"((((i) >> 1) & 1) ? voK1 : voK0), "s"(rs_k), "n"(((i) >> 2) * 256 + ((i) & 1) * 128) : "memory")
#define LDV(i) asm volatile("buffer_load_dwordx4 %0, %1, %2, %3 offen nt" : "+v"(vr[i]) : "v"(voV), "s"(rs_v), "s"((u32)((i) * 1024)) : "memory")
#define DMA_LANE(stride) ((u32)(lane >> 2) * (stride) + (u32)(((lane & 3) ^ (((lane >> 5) & 1) << 1)) * 16))
#define WF0() ((u32)((8 * g4 + (c16 >> 2)) * 64 + ((((c16 & 3) >> 1) ^ ((g4 & 1) << 1)) * 16) + (c16 & 1) * 8))
#define QS0(w) ((w) < 6 ? 3 * ((w) - 4) : 2 * (w) - 6)

  {
    STAGE_VARS();
    KV_DESC();
#pragma unroll
    for (int i = 0; i < 16; ++i) { asm volatile("" : "=a"(kr[i])); asm volatile("" : "=v"(vr[i])); }
    asm volatile("s_nop 4" ::: "memory");
    LDK(0); LDK(1);
    STAMP(0);
    const v4u sv = {SENT, SENT, SENT, SENT};
    if (w < 2) st16_sc1(sv, WS_S + c * 2048 + (w * 64 + lane) * 16, rs_ws);
    v2u gen;
    asm volatile("buffer_load_dwordx2 %0, %1, %2, 0 offen sc1" : "=v"(gen) : "v"(WS_G), "s"(rs_ws) : "memory");
    const rsrc_t rs_x = mk_rsrc(ka->x, 4 * DM * 2);
    const int slot = c16 >> 2, qs0 = QS0(w);
    const u32 o1 = w < 4 ? w * 1280 + lane * 16 : slot < (w < 6 ? 3 : 2) ? (c16 & 3) * 1280 + ((bh & 1) * 320 + (qs0 + slot) * 32 + 8 * g4) * 2 : OOB;
    const u32 o2 = (w < 4 && lane < 16) ? w * 1280 + 1024 + lane * 16 : OOB;
    asm volatile("s_nop 4\n\tbuffer_load_dwordx4 %0, %2, %4, 0 offen\n\tbuffer_load_dwordx4 %1, %3, %4, 0 offen"
                 : "=&v"(xa), "=&v"(xb) : "v"(o1), "v"(o2), "s"(rs_x) : "memory");
    if (w >= 4) {
      const rsrc_t rs = mk_rsrc(ka->wq + (size_t)(320 * (bh & 1) + 32 * qs0) * 4096 + h * 128 + 32 * (bh >> 1), 96 * 8192);
      const u32 vo = DMA_LANE(8192);
      asm volatile("s_nop 4" ::: "memory");
#pragma unroll
      for (int i = 0; i < 4; ++i) dma16(L_WQ + qs0 * 2048 + i * 1024, vo, rs, i * 16 * 8192);
      if (w < 6) { dma16(L_WQ + qs0 * 2048 + 4 * 1024, vo, rs, 4 * 16 * 8192); dma16(L_WQ + qs0 * 2048 + 5 * 1024, vo, rs, 5 * 16 * 8192); }
    }
    {
      const int s0 = w < 6 ? 7 * w : 42 + 6 * (w - 6), j4 = (g - 4) * 27 + r;
      const u16* base = role == 3 ? ka->wo + (size_t)(1024 * g + 128 * w) * DM + 32 * r
                      : role == 4 ? ((w & 1) ? ka->wu : ka->wg) + (size_t)(160 * (w >> 1)) * FF + 32 * j4
                                  : ka->wd + (size_t)(32 * s0) * DM + 32 * (r < 20 ? r : 0);
      const u32 stride = role == 4 ? 3456 : 1280;
      const u32 rows = role == 3 ? 128 : role == 4 ? 160 : role == 5 ? (w < 6 ? 224 : 192) : 0;
      rs_wr = mk_rsrc(base, rows * stride);
      wr_lane = DMA_LANE(stride);
      wr_step = 16 * stride;
      wr_lds = role == 3 ? L_WR + w * 8192 : role == 4 ? L_WR + (w & 1) * 40960 + (w >> 1) * 10240 : role == 5 ? L_WR + s0 * 2048 : L_WR + w * 8192;
    }
    __builtin_amdgcn_s_barrier();
    LDK(2); LDK(3); LDK(4); LDK(5); LDK(6); LDK(7); LDK(8); LDK(9);
    WAITVM(8);
    asm volatile("" : "+v"(gen), "+v"(xa), "+v"(xb));
    const u64 tag64 = (((u64)(u32)__builtin_amdgcn_readfirstlane(gen.y) << 32) | (u32)__builtin_amdgcn_readfirstlane(gen.x)) + 1ull;
    tag_lo = (u32)tag64; tag_hi = (u32)(tag64 >> 32);
    __builtin_amdgcn_s_barrier();
    if (w == 3) st8_sc1(v2u{tag_lo, tag_hi}, lane == 0 ? WS_FLAG + c * 8 : OOB, rs_ws);
  }

#pragma unroll 1
  for (int it = 0; it < 2; ++it) {
    {
      STAGE_VARS();
      const int qs0 = QS0(w);
      v4u wqf[6];
      if (w >= 4) {
        const u32 wf0 = WF0();
#pragma unroll
        for (int s = 0; s < 3; ++s) {
          const int sl = (s == 2 && w >= 6) ? qs0 : qs0 + s;
#pragma unroll
          for (int t = 0; t < 2; ++t) wqf[s * 2 + t] = wfrag(L + L_WQ + sl * 2048 + (wf0 ^ (t * 32)));
        }
#pragma unroll
        for (int i = 0; i < 6; ++i) asm volatile("" : "+a"(wqf[i]));
      } else {
#pragma unroll
        for (int i = 0; i < 6; ++i) asm volatile("" : "=a"(wqf[i]));
      }
      const u32 rl = red_lane(lane);
      const u32 qout = it == 0 ? ((lane & 1) ? OOB : WS_Q1 + c * 512 + (lane >> 1) * 16) : ((lane & 3) ? OOB : S_Q2 + (h * 8 + bh) * 256 + lane * 4);
      if (it == 1) {
        if (role == 0) idle<SLP_X_NONE>(); else if (role == 3) idle<SLP_X_S3>(); else if (role == 4) idle<SLP_X_S4>();
        const u32 base = S_X2 + g * 5120;
        const int slot = c16 >> 2;
        const u32 o1 = w < 4 ? base + w * 64 + (lane >> 2) * 256 + (lane & 3) * 16
                             : slot < (w < 6 ? 3 : 2) ? base + ((bh & 1) * 10 + qs0 + slot) * 256 + (c16 & 3) * 64 + g4 * 16 : OOB;
        const u32 o2 = (w < 4 && lane < 16) ? base + w * 64 + (16 + (lane >> 2)) * 256 + (lane & 3) * 16 : OOB;
        STAMP1(32);
        _Pragma("nounroll") for (u32 spin = 0; spin < SPIN_MAX; ++spin) {
          asm volatile("buffer_load_dwordx4 %0, %2, %4, 0 offen sc1\n\tbuffer_load_dwordx4 %1, %3, %4, 0 offen sc1\n\ts_waitcnt vmcnt(0)"
                       : "=&v"(xa), "=&v"(xb) : "v"(o1), "v"(o2), "s"(rs_ws) : "memory");
          STAMPVAL(40, spin + 1);
          if (all_valid(umax3(umax4(xa), umax4(xb), 0u))) break;
          idle<POLL_SLEEP>();
        }
      }
      STAMP(1 + it * 12);
      if (w < 4) {
        *(LAS v4u*)(L + L_XR + w * 1280 + lane * 16) = xa;
        if (lane < 16) *(LAS v4u*)(L + L_XR + w * 1280 + 1024 + lane * 16) = xb;
        const f2 st = row_stats(xa, xb);
        if (lane == 0) *(LAS f2*)(L + L_ST + w * 8) = st;
      } else {
        f4 acc0, acc1;
        asm volatile(
            MF("%0", "%2", "%8", "0") MVT("%9", "%10", "%11", "%12", "4") MF("%1", "%3", "%8", "0") MVU("%9", "%10", "%11", "%12", "8")
            MF("%0", "%4", RT, "%0") MF("%1", "%5", RT, "%1") MF("%0", "%6", RU, "%0") MF("%1", "%7", RU, "%1") "s_nop 7"
            : "=&v"(acc0), "=&v"(acc1)
            : "a"(wqf[0]), "a"(wqf[1]), "a"(wqf[2]), "a"(wqf[3]), "a"(wqf[4]), "a"(wqf[5]), "v"(xa), "v"(xa.x), "v"(xa.y), "v"(xa.z), "v"(xa.w)
            : TU_CLOBBER);
        if (c16 < 4) {
          *(LAS f4*)(L + L_RQ + (w - 4) * 512 + c16 * 64 + g4 * 16) = acc0;
          *(LAS f4*)(L + L_RQ + (w - 4) * 512 + 256 + c16 * 64 + g4 * 16) = acc1;
        }
        if (it == 0) {
          const v4u one = {ONES, ONES, ONES, ONES};
          const u32 z = w < 6 ? ONES : 0u;
          const v4u one3 = {z, z, z, z};
          f4 c0, c1;
          asm volatile("s_nop 1\n\t" MF("%0", "%2", "%8", "0") MF("%1", "%3", "%8", "0") MF("%0", "%4", "%8", "%0") MF("%1", "%5", "%8", "%1")
                       MF("%0", "%6", "%9", "%0") MF("%1", "%7", "%9", "%1") "s_nop 7"
                       : "=&v"(c0), "=&v"(c1)
                       : "a"(wqf[0]), "a"(wqf[1]), "a"(wqf[2]), "a"(wqf[3]), "a"(wqf[4]), "a"(wqf[5]), "v"(one), "v"(one3));
          if (c16 == 0) {
            *(LAS f4*)(L + L_CP + (w - 4) * 128 + g4 * 16) = c0;
            *(LAS f4*)(L + L_CP + (w - 4) * 128 + 64 + g4 * 16) = c1;
          }
        }
      }
      xa = v4u{0, 0, 0, 0}; xb = v4u{0, 0, 0, 0};
      lds_barrier();
      if (w == 0) {
        const f2 s = red_sum<4, 512>(L + L_RQ + rl);
        const f2 st = *(LAS f2*)(L + L_ST + g4 * 8);
        f2 cs;
        if (it == 0) { cs = red_sum<4, 128>(L + L_CP + c16 * 8); if (lane < 16) *(LAS f2*)(L + L_CS + c16 * 8) = cs; }
        else cs = *(LAS f2*)(L + L_CS + c16 * 8);
        const u32 d = nosent(cvtpk(st.y * fmaf(-st.x, cs.x, s.x), st.y * fmaf(-st.x, cs.y, s.y)));
        if (it == 0) st16_sc1(v4u{d, dppu<SHL1>(d), tag_lo, tag_hi}, qout, rs_ws);
        else st16_sc1(quad_gather(d), qout, rs_ws);
      }
      STAMP(2 + it * 12);
    }
    v2u qv;
    if (it == 0) {
      STAGE_VARS();
      KV_DESC();
      asm volatile("s_nop 4" ::: "memory");
      const u32 q1off = WS_Q1 + (g + 8 * ((h & 3) * 8 + (lane >> 3))) * 512 + b * 128 + (lane & 7) * 16;
      v4u p1, fl0, fl1;
#define W3 4
#define WN 3
#define THR() WAITVM_(WN)
#define KD(i) THR(); LDK(8 + i);
      THR(); LDK(10); THR(); LDK(11); THR(); LDK(12); THR(); LDK(13); THR(); LDK(14); THR(); LDK(15);
      THR(); LDV(0); THR(); LDV(1); THR(); LDV(2); THR(); LDV(3);
      asm volatile("buffer_load_dwordx4 %0, %1, %2, 0 offen sc1" : "=v"(p1) : "v"(q1off), "s"(rs_ws) : "memory");
      THR(); LDV(4); THR(); LDV(5); THR(); LDV(6); THR(); LDV(7);
      asm volatile("buffer_load_dwordx4 %0, %2, %3, 0 offen sc1\n\tbuffer_load_dwordx4 %1, %2, %3, 0 offen offset:1024 sc1"
                   : "=&v"(fl0), "=&v"(fl1) : "v"(w == 7 ? WS_FLAG + lane * 16 : OOB), "s"(rs_ws) : "memory");
      THR(); LDV(8); THR(); LDV(9); THR(); LDV(10); THR(); LDV(11); THR(); LDV(12); THR(); LDV(13); THR(); LDV(14);
      WAITVM(3); LDV(15);
      EARLY_DMA(0);
      STAMP(3);
      asm volatile("" : "+v"(p1), "+v"(fl0), "+v"(fl1));
      _Pragma("nounroll") for (u32 spin = 0; spin < SPIN_MAX; ++spin) {
        if (__builtin_amdgcn_ballot_w64(p1.z != tag_lo || p1.w != tag_hi) == 0ull) break;
        asm volatile("buffer_load_dwordx4 %0, %1, %2, 0 offen sc1\n\ts_waitcnt vmcnt(0)" : "=&v"(p1) : "v"(q1off), "s"(rs_ws) : "memory");
      }
      qv = v2u{p1.x, p1.y};
      if (w == 7) {
        _Pragma("nounroll") for (u32 spin = 0; spin < SPIN_MAX; ++spin) {
          const bool bad = fl0.x != tag_lo || fl0.y != tag_hi || fl0.z != tag_lo || fl0.w != tag_hi || fl1.x != tag_lo || fl1.y != tag_hi || fl1.z != tag_lo || fl1.w != tag_hi;
          if (__builtin_amdgcn_ballot_w64(bad) == 0ull) break;
          asm volatile("buffer_load_dwordx4 %0, %2, %3, 0 offen sc1\n\tbuffer_load_dwordx4 %1, %2, %3, 0 offen offset:1024 sc1\n\ts_waitcnt vmcnt(0)"
                       : "=&v"(fl0), "=&v"(fl1) : "v"(WS_FLAG + lane * 16), "s"(rs_ws) : "memory");
        }
      }
    } else {
      STAGE_VARS();
      const u32 off = S_Q2 + ((h * 8 + (lane >> 3)) * 4 + b) * 64 + (lane & 7) * 8;
      STAMP1(33);
      _Pragma("nounroll") for (u32 spin = 0; spin < SPIN_MAX; ++spin) {
        asm volatile("buffer_load_dwordx2 %0, %1, %2, 0 offen sc1\n\ts_waitcnt vmcnt(0)" : "=&v"(qv) : "v"(off), "s"(rs_ws) : "memory");
        STAMPVAL(41, spin + 1);
        if (all_valid(qv.x > qv.y ? qv.x : qv.y)) break;
        idle<POLL_SLEEP>();
      }
    }
    STAMP(4 + it * 12);
    CST(54);
    const u32 pbase = it * PASS_STRIDE;
    {
      STAGE_VARS();
      if (w < 4) __builtin_amdgcn_s_setprio(1);
      {
        float q0 = bflo(qv.x), q1 = bfhi(qv.x), q2 = bflo(qv.y), q3 = bfhi(qv.y);
        q0 += dpp<ROR8>(q0); q1 += dpp<ROR8>(q1); q2 += dpp<ROR8>(q2); q3 += dpp<ROR8>(q3);
        *(LAS v2u*)(L + L_QB + w * 256 + g4 * 64 + (lane & 7) * 8) = v2u{cvtpk(q0, q1), cvtpk(q2, q3)};
      }
      asm volatile("s_waitcnt lgkmcnt(0)" ::: "memory");
      const v4u qa0 = *(LAS v4u*)(L + L_QB + w * 256 + (c16 & 1) * 64 + g4 * 16);
      const v4u qa1 = *(LAS v4u*)(L + L_QB + w * 256 + 128 + (c16 & 1) * 64 + g4 * 16);
      const u32 ra_w = L_RA + w * 512 + c16 * 32 + g4 * 8;
      const u32 oout = (lane & 3) ? OOB : S_O0 + pbase + ((h * 2 + half) * 4 + b) * O_SLOT + lane * 4;
      const u32 sout = lane == 0 ? S_O0 + pbase + ((h * 2 + half) * 4 + b) * O_SLOT + 256 : OOB;
      float sraw;
      STAMPA(24);
      CST(55);
      {
        float sb;
#define SEL(d, s, rm, bm) "v_mov_b32_dpp " d ", " s " quad_perm:[0,1,2,3] row_mask:" rm " bank_mask:" bm "\n\t"
        asm volatile(
            MF("v[96:99]", "%2", "%4", "0") MF("v[100:103]", "%2", "%6", "0") MF("v[104:107]", "%2", "%8", "0") MF("v[108:111]", "%2", "%10", "0")
            MF("v[112:115]", "%2", "%12", "0") MF("v[116:119]", "%2", "%14", "0") MF("v[120:123]", "%2", "%16", "0") MF("v[124:127]", "%2", "%18", "0")
            MF("v[96:99]", "%3", "%5", "v[96:99]") MF("v[100:103]", "%3", "%7", "v[100:103]") MF("v[104:107]", "%3", "%9", "v[104:107]") MF("v[108:111]", "%3", "%11", "v[108:111]")
            MF("v[112:115]", "%3", "%13", "v[112:115]") MF("v[116:119]", "%3", "%15", "v[116:119]") MF("v[120:123]", "%3", "%17", "v[120:123]") MF("v[124:127]", "%3", "%19", "v[124:127]")
            "s_nop 0\n\tv_mov_b32 %0, v96\n\tv_mov_b32 %1, v100\n\t"
            SEL("%0", "v101", "0x1", "0xc") SEL("%1", "v97", "0x1", "0xc")
            SEL("%0", "v104", "0x2", "0x3") SEL("%1", "v108", "0x2", "0x3") SEL("%0", "v109", "0x2", "0xc") SEL("%1", "v105", "0x2", "0xc")
            SEL("%0", "v112", "0x4", "0x3") SEL("%1", "v116", "0x4", "0x3") SEL("%0", "v117", "0x4", "0xc") SEL("%1", "v113", "0x4", "0xc")
            SEL("%0", "v120", "0x8", "0x3") SEL("%1", "v124", "0x8", "0x3") SEL("%0", "v125", "0x8", "0xc") SEL("%1", "v121", "0x8", "0xc")
            "s_nop 1\n\tv_add_f32_dpp %0, %1, %0 row_ror:8 row_mask:0xf bank_mask:0xf"
            : "=&v"(sraw), "=&v"(sb)
            : "v"(qa0), "v"(qa1), "a"(kr[0]), "a"(kr[1]), "a"(kr[2]), "a"(kr[3]), "a"(kr[4]), "a"(kr[5]), "a"(kr[6]), "a"(kr[7]),
              "a"(kr[8]), "a"(kr[9]), "a"(kr[10]), "a"(kr[11]), "a"(kr[12]), "a"(kr[13]), "a"(kr[14]), "a"(kr[15])
            : "v96", "v97", "v98", "v99", "v100", "v101", "v102", "v103", "v104", "v105", "v106", "v107", "v108", "v109", "v110", "v111",
              "v112", "v113", "v114", "v115", "v116", "v117", "v118", "v119", TU_CLOBBER);
      }
      STAMPA(25);
      CST(56);
      EARLY_DMA(1);
      const float s2 = sraw * (0.08838834764831845f * 1.4426950408889634f);
      const float mw = wave_max(s2);
      const float pe = __builtin_amdgcn_exp2f(s2 - mw);
      const u32 pp = cvtpk(pe, dpp<SHL1>(pe));
      const float lw = wave_sum(pe);
      float oc[8] = {0, 0, 0, 0, 0, 0, 0, 0};
      STAMPA(26);
      CST(57);
      EARLY_DMA(2);
#define VP1(a, b) "v_perm_b32 %8, " b ", " a ", %9\n\tv_perm_b32 " b ", " b ", " a ", %10\n\tv_mov_b32 " a ", %8\n\t"
#define VPAIR(m) do { u32 t_; asm volatile(VP1("%0", "%4") VP1("%1", "%5") VP1("%2", "%6") VP1("%3", "%7") \
      : "+v"(vr[2 * (m)][0]), "+v"(vr[2 * (m)][1]), "+v"(vr[2 * (m)][2]), "+v"(vr[2 * (m)][3]), \
        "+v"(vr[2 * (m) + 1][0]), "+v"(vr[2 * (m) + 1][1]), "+v"(vr[2 * (m) + 1][2]), "+v"(vr[2 * (m) + 1][3]), "=&v"(t_) \
      : "s"(0x05040100u), "s"(0x07060302u)); } while (0)
#define D2(d, v, bc) "v_dot2c_f32_bf16_dpp " d ", %8, " v " row_newbcast:" #bc " row_mask:0xf bank_mask:0xf\n\t"
#define PV(m, bc, pre, post) asm volatile(pre D2("%0", "%9", bc) D2("%1", "%13", bc) D2("%2", "%10", bc) D2("%3", "%14", bc) \
      D2("%4", "%11", bc) D2("%5", "%15", bc) D2("%6", "%12", bc) D2("%7", "%16", bc) post \
      : "+v"(oc[0]), "+v"(oc[1]), "+v"(oc[2]), "+v"(oc[3]), "+v"(oc[4]), "+v"(oc[5]), "+v"(oc[6]), "+v"(oc[7]) \
      : "v"(pp), "v"(vr[2 * (m)][0]), "v"(vr[2 * (m)][1]), "v"(vr[2 * (m)][2]), "v"(vr[2 * (m)][3]), \
        "v"(vr[2 * (m) + 1][0]), "v"(vr[2 * (m) + 1][1]), "v"(vr[2 * (m) + 1][2]), "v"(vr[2 * (m) + 1][3]))
      if (it == 0) {
#pragma unroll
        for (int i = 0; i < 12; ++i) asm volatile("" : "+v"(vr[i]));
        VPAIR(0); VPAIR(1); VPAIR(2); VPAIR(3); VPAIR(4); VPAIR(5);
      }
      EARLY_DMA(3);
      PV(0, 0, "s_nop 1\n\t", ""); PV(1, 2, "", "");
      EARLY_DMA(4);
      PV(2, 4, "", ""); PV(3, 6, "", "");
      EARLY_DMA(5);
      PV(4, 8, "", ""); PV(5, 10, "", "");
      EARLY_DMA(6);
      if (it == 0) {
        WAIT_EARLY(2, 7);
        asm volatile("" : "+v"(vr[12]), "+v"(vr[13]));
        VPAIR(6);
      }
      PV(6, 12, "", "");
      EARLY_DMA(7);
      if (it == 0) {
        WAIT_EARLY(0, 8);
        asm volatile("" : "+v"(vr[14]), "+v"(vr[15]));
        VPAIR(7);
      }
      PV(7, 14, "", "s_nop 2");
      STAMP(5 + it * 12);
      CST(58);
      float t4[4], u2[2];
#pragma unroll
      for (int d = 0; d < 4; ++d) {
        auto s = __builtin_amdgcn_permlane32_swap(__float_as_uint(oc[d]), __float_as_uint(oc[d + 4]), false, false);
        t4[d] = __uint_as_float(s[0]) + __uint_as_float(s[1]);
      }
#pragma unroll
      for (int d = 0; d < 2; ++d) {
        auto s = __builtin_amdgcn_permlane16_swap(__float_as_uint(t4[d]), __float_as_uint(t4[d + 2]), false, false);
        u2[d] = __uint_as_float(s[0]) + __uint_as_float(s[1]);
      }
      *(LAS f2*)(L + ra_w) = f2{u2[0], u2[1]};
      if (lane == 0) *(LAS f2*)(L + L_RA + 4096 + w * 8) = f2{mw, lw};
      __builtin_amdgcn_s_setprio(0);
      asm volatile("s_waitcnt lgkmcnt(0)" ::: "memory");
      CST(59);
      lds_barrier();
      CST(60);
      STAMPA(27);
      if (w == 0) {
        const f2 st = *(LAS f2*)(L + L_RA + 4096 + (lane & 7) * 8);
        f2 v[8];
#pragma unroll
        for (int i = 0; i < 8; ++i) v[i] = *(LAS f2*)(L + L_RA + i * 512 + lane * 8);
        float M = fmaxf(st.x, dpp<0xB1>(st.x)); M = fmaxf(M, dpp<0x4E>(M)); M = fmaxf(M, dpp<0x141>(M));
        const float f = __builtin_amdgcn_exp2f(st.x - M);
        float Ls = f * st.y; Ls += dpp<0xB1>(Ls); Ls += dpp<0x4E>(Ls); Ls += dpp<0x141>(Ls);
        const float fn = f * __builtin_amdgcn_rcpf(Ls);
        float o0 = 0.f, o1 = 0.f;
#define MRG(i) "v_fmac_f32_dpp %0, %2, %" #i " row_newbcast:"
        asm volatile("s_nop 1\n\t"
                     "v_fmac_f32_dpp %0, %2, %3 row_newbcast:0 row_mask:0xf bank_mask:0xf\n\tv_fmac_f32_dpp %1, %2, %4 row_newbcast:0 row_mask:0xf bank_mask:0xf\n\t"
                     "v_fmac_f32_dpp %0, %2, %5 row_newbcast:1 row_mask:0xf bank_mask:0xf\n\tv_fmac_f32_dpp %1, %2, %6 row_newbcast:1 row_mask:0xf bank_mask:0xf\n\t"
                     "v_fmac_f32_dpp %0, %2, %7 row_newbcast:2 row_mask:0xf bank_mask:0xf\n\tv_fmac_f32_dpp %1, %2, %8 row_newbcast:2 row_mask:0xf bank_mask:0xf\n\t"
                     "v_fmac_f32_dpp %0, %2, %9 row_newbcast:3 row_mask:0xf bank_mask:0xf\n\tv_fmac_f32_dpp %1, %2, %10 row_newbcast:3 row_mask:0xf bank_mask:0xf\n\t"
                     "v_fmac_f32_dpp %0, %2, %11 row_newbcast:4 row_mask:0xf bank_mask:0xf\n\tv_fmac_f32_dpp %1, %2, %12 row_newbcast:4 row_mask:0xf bank_mask:0xf\n\t"
                     "v_fmac_f32_dpp %0, %2, %13 row_newbcast:5 row_mask:0xf bank_mask:0xf\n\tv_fmac_f32_dpp %1, %2, %14 row_newbcast:5 row_mask:0xf bank_mask:0xf\n\t"
                     "v_fmac_f32_dpp %0, %2, %15 row_newbcast:6 row_mask:0xf bank_mask:0xf\n\tv_fmac_f32_dpp %1, %2, %16 row_newbcast:6 row_mask:0xf bank_mask:0xf\n\t"
                     "v_fmac_f32_dpp %0, %2, %17 row_newbcast:7 row_mask:0xf bank_mask:0xf\n\tv_fmac_f32_dpp %1, %2, %18 row_newbcast:7 row_mask:0xf bank_mask:0xf"
                     : "+v"(o0), "+v"(o1)
                     : "v"(fn), "v"(v[0].x), "v"(v[0].y), "v"(v[1].x), "v"(v[1].y), "v"(v[2].x), "v"(v[2].y), "v"(v[3].x), "v"(v[3].y),
                       "v"(v[4].x), "v"(v[4].y), "v"(v[5].x), "v"(v[5].y), "v"(v[6].x), "v"(v[6].y), "v"(v[7].x), "v"(v[7].y));
        CST(61);
        const u32 d = nosent(cvtpk(o0, o1));
        st16_sc1(quad_gather(d), oout, rs_ws);
        CST(62);
        st8_sc1(v2u{nosent(__float_as_uint(M)), nosent(__float_as_uint(Ls))}, sout, rs_ws);
      }
      STAMP(6 + it * 12);
    }
    if (role == 3) {
      STAGE_VARS();
      const int hh = 8 * g + w;
      const u32 sl = S_O0 + pbase + (hh * 8 + (c16 & 3)) * O_SLOT;
      const u32 o0off = sl + (c16 >> 2) * 64 + g4 * 16, stoff = sl + 256;
      v4u oa, ob;
      v2u s0, s1;
#define POLL3(wait) asm volatile("buffer_load_dwordx4 %0, %4, %6, 0 offen sc1\n\tbuffer_load_dwordx4 %1, %4, %6, 0 offen offset:1536 sc1\n\t" \
                   "buffer_load_dwordx2 %2, %5, %6, 0 offen sc1\n\tbuffer_load_dwordx2 %3, %5, %6, 0 offen offset:1536 sc1\n\ts_waitcnt vmcnt(" wait ")" \
                   : "=&v"(oa), "=&v"(ob), "=&v"(s0), "=&v"(s1) : "v"(o0off), "v"(stoff), "s"(rs_ws) : "memory")
      if (it == 0) {
        if (NE3 < 8) {
          lds_barrier();
#pragma unroll
          for (int i = NE3; i < 8; ++i) ROLE_DMA(i);
        }
        STAMP(12);
        WAITVM(0);
        STAMP(15);
      }
      v4u wf[8];
      const u32 wf0 = WF0();
#pragma unroll
      for (int s = 0; s < 4; ++s)
#pragma unroll
        for (int t = 0; t < 2; ++t) wf[s * 2 + t] = wfrag(L + L_WR + w * 8192 + s * 2048 + (wf0 ^ (t * 32)));
      const u32 rl = red_lane(lane), rw = L_RR + w * 512 + c16 * 64 + g4 * 16;
      const u32 xout = (lane & 3) < 2 ? S_X1P0 + pbase + (lane & 3) * 20480 + (g * 20 + r) * 256 + (lane >> 2) * 16 : OOB;
      const u32 xres = L_XR + g4 * 1280 + (32 * r + 2 * c16) * 2;
      STAMP1(34);
      POLL3("0");
      _Pragma("nounroll") for (u32 spin = 0; spin < SPIN_MAX; ++spin) {
        STAMPVAL(42, spin + 1);
        if (all_valid(umax3(umax3(umax4(oa), umax4(ob), s0.x), umax3(s0.y, s1.x, s1.y), 0u))) break;
        idle<POLL_SLEEP>();
        POLL3("0");
      }
#pragma unroll
      for (int i = 0; i < 8; ++i) asm volatile("" : "+a"(wf[i]));
      STAMP(7 + it * 12);
      const float m0 = __uint_as_float(s0.x), l0 = __uint_as_float(s0.y), m1 = __uint_as_float(s1.x), l1 = __uint_as_float(s1.y);
      const float mx = fmaxf(m0, m1);
      const float w0 = l0 * __builtin_amdgcn_exp2f(m0 - mx), w1 = l1 * __builtin_amdgcn_exp2f(m1 - mx);
      const float inv = __builtin_amdgcn_rcpf(w0 + w1), a0 = w0 * inv, a1 = w1 * inv;
      v4u of;
#pragma unroll
      for (int j = 0; j < 4; ++j) of[j] = cvtpk(fmaf(a0, bflo(oa[j]), a1 * bflo(ob[j])), fmaf(a0, bfhi(oa[j]), a1 * bfhi(ob[j])));
      f4 acc0, acc1;
      asm volatile("s_nop 1\n\t"
          MF("%0", "%2", "%10", "0") MVT("%11", "%12", "%13", "%14", "4") MF("%1", "%3", "%10", "0") MVU("%11", "%12", "%13", "%14", "8")
          MF("%0", "%4", RT, "%0") MF("%1", "%5", RT, "%1") MVT("%11", "%12", "%13", "%14", "12")
          MF("%0", "%6", RU, "%0") MF("%1", "%7", RU, "%1") MF("%0", "%8", RT, "%0") MF("%1", "%9", RT, "%1") "s_nop 7"
          : "=&v"(acc0), "=&v"(acc1)
          : "a"(wf[0]), "a"(wf[1]), "a"(wf[2]), "a"(wf[3]), "a"(wf[4]), "a"(wf[5]), "a"(wf[6]), "a"(wf[7]),
            "v"(of), "v"(of.x), "v"(of.y), "v"(of.z), "v"(of.w)
          : TU_CLOBBER);
      STAMP1(28);
      if (c16 < 4) { *(LAS f4*)(L + rw) = acc0; *(LAS f4*)(L + rw + 256) = acc1; }
      lds_barrier();
      STAMP1(29);
      if (w == 0) {
        f2 s = red_sum<8, 512>(L + L_RR + rl);
        if (g == 0) { const u32 x = *(LAS u32*)(L + xres); s.x += bflo(x); s.y += bfhi(x); }
        st16_sc1(quad_gather(nosent(cvtpk(s.x, s.y))), xout, rs_ws);
      }
    } else if (role == 4) {
      STAGE_VARS();
      const int j4 = (g - 4) * 27 + r;
      const u32 wf0 = WF0() ^ ((w & 1) * 32);
      const u32 wbase = L_WR + ((w >> 1) & 1) * 40960 + (w >> 2) * 20480;
      v4u wf[10];
      if (it == 0) {
        lds_barrier();
#pragma unroll
        for (int i = NE; i < 10; ++i) ROLE_DMA(i);
        WAITVM(0);
        lds_barrier();
#pragma unroll
        for (int s = 0; s < 10; ++s) wf[s] = wfrag(L + wbase + s * 2048 + wf0);
#pragma unroll
        for (int i = 0; i < 10; ++i) asm volatile("" : "+a"(wf[i]));
        const v4u one = {ONES, ONES, ONES, ONES};
        f4 c0;
        asm volatile("s_nop 1\n\t" MF("%0", "%1", "%11", "0") MF("%0", "%2", "%11", "%0") MF("%0", "%3", "%11", "%0") MF("%0", "%4", "%11", "%0") MF("%0", "%5", "%11", "%0")
                     MF("%0", "%6", "%11", "%0") MF("%0", "%7", "%11", "%0") MF("%0", "%8", "%11", "%0") MF("%0", "%9", "%11", "%0") MF("%0", "%10", "%11", "%0") "s_nop 7"
                     : "=&v"(c0)
                     : "a"(wf[0]), "a"(wf[1]), "a"(wf[2]), "a"(wf[3]), "a"(wf[4]), "a"(wf[5]), "a"(wf[6]), "a"(wf[7]), "a"(wf[8]), "a"(wf[9]), "v"(one));
        if (c16 == 0) *(LAS f4*)(L + L_CP + (w >> 2) * 256 + (w & 3) * 64 + g4 * 16) = c0;
        lds_barrier();
        if (w == 1 && lane < 32) *(LAS f2*)(L + L_CS + 128 + lane * 8) = *(LAS f2*)(L + L_CP + lane * 8) + *(LAS f2*)(L + L_CP + 256 + lane * 8);
      } else {
        idle<SLP_S4>();
#pragma unroll
        for (int s = 0; s < 10; ++s) wf[s] = wfrag(L + wbase + s * 2048 + wf0);
#pragma unroll
        for (int i = 0; i < 10; ++i) asm volatile("" : "+a"(wf[i]));
      }
      const u32 rl = red_lane(lane);
      const u32 hout = (lane & 3) ? OOB : S_HB0 + pbase + j4 * 256 + lane * 4;
      if (w < 5) {
        const u32 u = w * 64 + lane;
        const u32 o1 = S_X1P0 + pbase + (g - 4) * 20480 + u * 16;
        const u32 xw = L_XN + ((u >> 2) & 3) * XN_STRIDE + (u >> 4) * 64 + (u & 3) * 16;
        v4u p0, p1, p2, p3;
        STAMP1(35);
        _Pragma("nounroll") for (u32 spin = 0; spin < SPIN_MAX; ++spin) {
          STAMPVAL(43, spin + 1);
          asm volatile("buffer_load_dwordx4 %0, %4, %5, 0 offen sc1\n\tbuffer_load_dwordx4 %1, %4, %5, %6 offen sc1\n\t"
                       "buffer_load_dwordx4 %2, %4, %5, %7 offen sc1\n\tbuffer_load_dwordx4 %3, %4, %5, %8 offen sc1\n\ts_waitcnt vmcnt(0)"
                       : "=&v"(p0), "=&v"(p1), "=&v"(p2), "=&v"(p3)
                       : "v"(o1), "s"(rs_ws), "s"(5120u), "s"(10240u), "s"(15360u) : "memory");
          if (all_valid(umax3(umax3(umax4(p0), umax4(p1), 0u), umax4(p2), umax4(p3)))) break;
          idle<POLL_SLEEP>();
        }
        STAMP(8 + it * 12);
        v4u xs;
        u32 sel_lo = 0x00003F80u, sel_hi = 0x3F800000u;
        LAUNDER_S(sel_lo); LAUNDER_S(sel_hi);
#pragma unroll
        for (int j = 0; j < 4; ++j) {
          float lo = dot2(p0[j], sel_lo, 0.f), hi = dot2(p0[j], sel_hi, 0.f);
          lo = dot2(p1[j], sel_lo, lo); hi = dot2(p1[j], sel_hi, hi);
          lo = dot2(p2[j], sel_lo, lo); hi = dot2(p2[j], sel_hi, hi);
          lo = dot2(p3[j], sel_lo, lo); hi = dot2(p3[j], sel_hi, hi);
          xs[j] = cvtpk(lo, hi);
        }
        *(LAS v4u*)(L + xw) = xs;
      }
      STAMPB(24);
      lds_barrier();
      STAMPB(25);
      const u32 xaddr = L_XN + (c16 & 3) * XN_STRIDE + ((w >> 2) * 320 + 8 * g4) * 2;
      f4 acc;
#define XRD(d, off) "ds_read_b128 " d ", %11 offset:" off "\n\t"
#define LGK(n) "s_waitcnt lgkmcnt(" n ")\n\t"
#define S4_HEAD XRD("v[96:99]", "0") XRD("v[100:103]", "64") XRD("v[104:107]", "128") XRD("v[108:111]", "192") \
                XRD("v[112:115]", "256") XRD("v[116:119]", "320") XRD("v[120:123]", "384") XRD("v[124:127]", "448")
#define S4_CLOBBER "v96", "v97", "v98", "v99", "v100", "v101", "v102", "v103", "v104", "v105", "v106", "v107", "v108", "v109", "v110", "v111", \
                   "v112", "v113", "v114", "v115", "v116", "v117", "v118", "v119", TU_CLOBBER, "memory"
#define S4_W "a"(wf[0]), "a"(wf[1]), "a"(wf[2]), "a"(wf[3]), "a"(wf[4]), "a"(wf[5]), "a"(wf[6]), "a"(wf[7]), "a"(wf[8]), "a"(wf[9]), "v"(xaddr)
      if (w < 4) {
        const v4u ra = *(LAS v4u*)(L + L_XN + w * XN_STRIDE + lane * 16);
        v4u rb = *(LAS v4u*)(L + L_XN + w * XN_STRIDE + 1024 + (lane & 15) * 16);
        if (lane >= 16) rb = v4u{0, 0, 0, 0};
        float s1 = 0.f, s2 = 0.f, t1, t2;
#define DS1(x) "v_dot2c_f32_bf16 %1, %24, " x "\n\tv_dot2c_f32_bf16 %2, " x ", " x "\n\t"
#define RED2(ctl) "v_add_f32_dpp %1, %1, %1 " ctl " row_mask:0xf bank_mask:0xf\n\tv_add_f32_dpp %2, %2, %2 " ctl " row_mask:0xf bank_mask:0xf\n\t"
#define SWP2(op) "v_mov_b32 %3, %1\n\tv_mov_b32 %4, %2\n\ts_nop 1\n\t" op " %1, %3\n\t" op " %2, %4\n\tv_add_f32 %1, %1, %3\n\tv_add_f32 %2, %2, %4\n\t"
        asm volatile(
            "ds_read_b128 v[96:99], %15 offset:0\n\tds_read_b128 v[100:103], %15 offset:64\n\tds_read_b128 v[104:107], %15 offset:128\n\tds_read_b128 v[108:111], %15 offset:192\n\t"
            "ds_read_b128 v[112:115], %15 offset:256\n\tds_read_b128 v[116:119], %15 offset:320\n\tds_read_b128 v[120:123], %15 offset:384\n\tds_read_b128 v[124:127], %15 offset:448\n\t"
            DS1("%16") DS1("%17") LGK("7") MF("%0", "%5", "v[96:99]", "0") "ds_read_b128 v[96:99], %15 offset:512\n\t"
            DS1("%18") DS1("%19") LGK("7") MF("%0", "%6", "v[100:103]", "%0") "ds_read_b128 v[100:103], %15 offset:576\n\t"
            DS1("%20") DS1("%21") LGK("7") MF("%0", "%7", "v[104:107]", "%0")
            DS1("%22") DS1("%23") LGK("6") MF("%0", "%8", "v[108:111]", "%0")
            "s_nop 1\n\t" RED2("row_ror:8") LGK("5") MF("%0", "%9", "v[112:115]", "%0")
            RED2("row_ror:4") LGK("4") MF("%0", "%10", "v[116:119]", "%0")
            RED2("row_ror:2") LGK("3") MF("%0", "%11", "v[120:123]", "%0")
            RED2("row_ror:1") LGK("2") MF("%0", "%12", "v[124:127]", "%0")
            SWP2("v_permlane16_swap_b32") LGK("1") MF("%0", "%13", "v[96:99]", "%0")
            SWP2("v_permlane32_swap_b32") LGK("0") MF("%0", "%14", "v[100:103]", "%0")
            : "=&v"(acc), "+v"(s1), "+v"(s2), "=&v"(t1), "=&v"(t2)
            : S4_W, "v"(ra.x), "v"(ra.y), "v"(ra.z), "v"(ra.w), "v"(rb.x), "v"(rb.y), "v"(rb.z), "v"(rb.w), "s"(ONES)
            : S4_CLOBBER);
        const float mean = s1 * (1.f / DM);
        const float var = fmaxf(fmaf(-mean, mean, s2 * (1.f / DM)), 0.f);
        if (lane == 0) *(LAS f2*)(L + L_ST + w * 8) = f2{mean, __builtin_amdgcn_rsqf(var + 1e-5f)};
      } else {
        asm volatile(S4_HEAD
            LGK("7") MF("%0", "%1", "v[96:99]", "0") XRD("v[96:99]", "512")
            LGK("7") MF("%0", "%2", "v[100:103]", "%0") XRD("v[100:103]", "576")
            LGK("7") MF("%0", "%3", "v[104:107]", "%0") LGK("6") MF("%0", "%4", "v[108:111]", "%0")
            LGK("5") MF("%0", "%5", "v[112:115]", "%0") LGK("4") MF("%0", "%6", "v[116:119]", "%0")
            LGK("3") MF("%0", "%7", "v[120:123]", "%0") LGK("2") MF("%0", "%8", "v[124:127]", "%0")
            LGK("1") MF("%0", "%9", "v[96:99]", "%0") LGK("0") MF("%0", "%10", "v[100:103]", "%0")
            : "=&v"(acc) : S4_W : S4_CLOBBER);
      }
      asm volatile("s_nop 7" : "+v"(acc));
      STAMPB(26);
      if (c16 < 4) *(LAS f4*)(L + L_RR + (w >> 2) * 1024 + ((w >> 1) & 1) * 512 + (w & 1) * 256 + c16 * 64 + g4 * 16) = acc;
      lds_barrier();
      STAMPB(27);
      if (w == 0) {
        const f2 ga = red_sum<2, 1024>(L + L_RR + rl), up = red_sum<2, 1024>(L + L_RR + 512 + rl);
        const f2 st = *(LAS f2*)(L + L_ST + g4 * 8);
        const f2 cg = *(LAS f2*)(L + L_CS + 128 + c16 * 8), cu = *(LAS f2*)(L + L_CS + 256 + c16 * 8);
        const float g0 = st.y * fmaf(-st.x, cg.x, ga.x), g1 = st.y * fmaf(-st.x, cg.y, ga.y);
        const float u0 = st.y * fmaf(-st.x, cu.x, up.x), u1 = st.y * fmaf(-st.x, cu.y, up.y);
        const u32 d = nosent(cvtpk((g0 > 0.f ? g0 : 0.f * g0) * u0, (g1 > 0.f ? g1 : 0.f * g1) * u1));
        st16_sc1(quad_gather(d), hout, rs_ws);
      }
    } else if (role == 5) {
      STAGE_VARS();
      const int s0 = w < 6 ? 7 * w : 42 + 6 * (w - 6);
      if (it == 0) {
        lds_barrier();
#pragma unroll
        for (int i = NE; i < 12; ++i) ROLE_DMA(i);
        if (w < 6) { ROLE_DMA(12); ROLE_DMA(13); }
        WAITVM(0);
      }
      idle<SLP_S5>();
      v4u wf[14];
      const u32 wf0 = WF0();
#pragma unroll
      for (int s = 0; s < 6; ++s)
#pragma unroll
        for (int t = 0; t < 2; ++t) wf[s * 2 + t] = wfrag(L + L_WR + (s0 + s) * 2048 + (wf0 ^ (t * 32)));
      {
        const int s6 = w < 6 ? s0 + 6 : s0;
        wf[12] = wfrag(L + L_WR + s6 * 2048 + wf0); wf[13] = wfrag(L + L_WR + s6 * 2048 + (wf0 ^ 32));
      }
#pragma unroll
      for (int i = 0; i < 14; ++i) asm volatile("" : "+a"(wf[i]));
      const int slot = c16 >> 2;
      const u32 hoff = S_HB0 + pbase + (c16 & 3) * 64 + g4 * 16;
      const u32 oA = hoff + (s0 + slot) * 256;
      const u32 oB = (slot < (w < 6 ? 3 : 2)) ? hoff + (s0 + 4 + slot) * 256 : OOB;
      v4u ha, hb;
      u32 rx0, rx1, rx2, rx3;
      const u32 rxo = w == 7 ? S_X1P0 + pbase + r * 256 + lane * 4 : OOB;
      const u32 rl = red_lane(lane), rw = L_RR + w * 512 + c16 * 64 + g4 * 16;
      const u32 xout = it == 0 ? S_X2 + (lane & 3) * 5120 + r * 256 + (lane >> 2) * 16 : ((lane & 3) ? OOB : (u32)(g4 * 1280 + r * 64 + (c16 >> 2) * 16));
      STAMP1(36);
      _Pragma("nounroll") for (u32 spin = 0; spin < SPIN_MAX; ++spin) {
        STAMPVAL(44, spin + 1);
        asm volatile("buffer_load_dwordx4 %0, %6, %9, 0 offen sc1\n\tbuffer_load_dwordx4 %1, %7, %9, 0 offen sc1\n\t"
                     "buffer_load_dword %2, %8, %9, 0 offen sc1\n\tbuffer_load_dword %3, %8, %9, %10 offen sc1\n\tbuffer_load_dword %4, %8, %9, %11 offen sc1\n\tbuffer_load_dword %5, %8, %9, %12 offen sc1\n\ts_waitcnt vmcnt(0)"
                     : "=&v"(ha), "=&v"(hb), "=&v"(rx0), "=&v"(rx1), "=&v"(rx2), "=&v"(rx3)
                     : "v"(oA), "v"(oB), "v"(rxo), "s"(rs_ws), "s"(5120u), "s"(10240u), "s"(15360u) : "memory");
        if (all_valid(umax3(umax3(umax4(ha), umax4(hb), rx0), umax3(rx1, rx2, rx3), 0u))) break;
        idle<POLL_SLEEP>();
      }
      STAMP(9 + it * 12);
      CST(48);
      f4 acc0, acc1;
      asm volatile(
          MF("%0", "%2", "%16", "0") MVT("%18", "%19", "%20", "%21", "4") MF("%1", "%3", "%16", "0") MVU("%18", "%19", "%20", "%21", "8")
          MF("%0", "%4", RT, "%0") MF("%1", "%5", RT, "%1") MVT("%18", "%19", "%20", "%21", "12")
          MF("%0", "%6", RU, "%0") MF("%1", "%7", RU, "%1") MVU("%22", "%23", "%24", "%25", "4")
          MF("%0", "%8", RT, "%0") MF("%1", "%9", RT, "%1") MVT("%22", "%23", "%24", "%25", "8")
          MF("%0", "%10", "%17", "%0") MF("%1", "%11", "%17", "%1") MF("%0", "%12", RU, "%0") MF("%1", "%13", RU, "%1")
          MF("%0", "%14", RT, "%0") MF("%1", "%15", RT, "%1") "s_nop 7"
          : "=&v"(acc0), "=&v"(acc1)
          : "a"(wf[0]), "a"(wf[1]), "a"(wf[2]), "a"(wf[3]), "a"(wf[4]), "a"(wf[5]), "a"(wf[6]), "a"(wf[7]), "a"(wf[8]), "a"(wf[9]),
            "a"(wf[10]), "a"(wf[11]), "a"(wf[12]), "a"(wf[13]), "v"(ha), "v"(hb),
            "v"(ha.x), "v"(ha.y), "v"(ha.z), "v"(ha.w), "v"(hb.x), "v"(hb.y), "v"(hb.z), "v"(hb.w)
          : TU_CLOBBER);
      STAMP1(30);
      CST(49);
      if (c16 < 4) { *(LAS f4*)(L + rw) = acc0; *(LAS f4*)(L + rw + 256) = acc1; }
      if (w == 7)
        *(LAS f2*)(L + L_RR + 8 * 512 + rl) =
            f2{(bflo(rx0) + bflo(rx1)) + (bflo(rx2) + bflo(rx3)), (bfhi(rx0) + bfhi(rx1)) + (bfhi(rx2) + bfhi(rx3))};
      asm volatile("s_waitcnt lgkmcnt(0)" ::: "memory");
      CST(50);
      lds_barrier();
      CST(51);
      STAMP1(31);
      if (w == 0) {
        f2 s = red_sum<9, 512>(L + L_RR + rl);
        CST(52);
        const u32 d0 = cvtpk(s.x, s.y);
        if (it == 0) {
          const v4u q = quad_gather(nosent(d0));
          st16_sc1(q, xout, rs_ws);
          st16_sc1(q, xout + 4 * 5120, rs_ws);
        } else {
          const v4u q = quad_gather(d0);
          const rsrc_t rs_o = mk_rsrc(outp, 4 * DM * 2);
          asm volatile("s_nop 4\n\tbuffer_store_dwordx4 %0, %1, %2, 0 offen\n\ts_nop 1" ::"v"(q), "v"(xout), "s"(rs_o) : "memory");
          CST(53);
          if (r == 0) st8_sc1(v2u{tag_lo, tag_hi}, lane == 0 ? WS_G : OOB, rs_ws);
        }
      }
    }
    STAMP(10 + it * 12);
  }
  STAMP_OUT();
}

}

extern "C" void kernel_launch(void* const* d_in, const int* in_sizes, int n_in, void* d_out, int out_size, void* d_ws,
                              size_t ws_size, hipStream_t stream) {
  Args a;
  a.x = (const u16*)d_in[0];
  a.k = (const u16*)d_in[1];
  a.v = (const u16*)d_in[2];
  a.wq = (const u16*)d_in[3];
  a.wo = (const u16*)d_in[4];
  a.wg = (const u16*)d_in[6];
  a.wu = (const u16*)d_in[7];
  a.wd = (const u16*)d_in[8];
  a.out = (u16*)d_out;
  a.ws = (unsigned char*)d_ws;
  (void)hipFuncSetAttribute((const void*)fused_decode, hipFuncAttributeMaxDynamicSharedMemorySize, (int)L_END);
  hipLaunchKernelGGL(fused_decode, dim3(NWG), dim3(NT), L_END, stream, a);
}
```

```cpp
#include <hip/hip_runtime.h>
#include <hip/hip_bf16.h>

typedef unsigned int u32;
typedef unsigned long long u64;
typedef unsigned short u16;
typedef u32 v4u __attribute__((ext_vector_type(4)));
typedef u32 v2u __attribute__((ext_vector_type(2)));
typedef float f4 __attribute__((ext_vector_type(4)));
typedef float f2 __attribute__((ext_vector_type(2)));
typedef __bf16 bf16x8 __attribute__((ext_vector_type(8)));
typedef short s16x4 __attribute__((ext_vector_type(4)));
#define LAS __attribute__((address_space(3)))

namespace {

constexpr int NWG = 256, NT = 512;
constexpr int DM = 640, SK = 4096, WIN = 1024, DH = 128, FF = 1728;

constexpr u32 WS_G = 0;
constexpr u32 WS_FLAG = 4096;
constexpr u32 WS_Q1 = 8192;
constexpr u32 WS_S = 262144;
constexpr u32 O_SLOT = 384;
constexpr u32 S_O0 = WS_S, S_X1P0 = S_O0 + 256 * O_SLOT, S_HB0 = S_X1P0 + 40960, S_X2 = S_HB0 + 13824;
constexpr u32 S_O1 = S_X2 + 40960;
constexpr u32 PASS_STRIDE = S_O1 - S_O0;
constexpr u32 S_Q2 = S_O1 + (S_X2 - S_O0);
constexpr u32 WS_END = WS_S + 256 * 2048;
static_assert(S_Q2 + 65536 <= WS_END, "sentinel region too small");
static_assert((S_X1P0 % 128) == 0 && (S_X2 % 128) == 0 && (S_O1 % 128) == 0 && (S_Q2 % 128) == 0 && (S_HB0 % 128) == 0, "alignment");
constexpr u32 WS_BYTES = 4u << 20;
constexpr u32 OOB = 0x80000000u;

constexpr u32 L_WR = 0;
constexpr u32 L_WQ = 110592;
constexpr u32 L_XN = 131072;
constexpr u32 XN_STRIDE = 1344;
constexpr u32 L_XR = L_XN + 4 * XN_STRIDE;
constexpr u32 L_RA = L_XR + 5120;
constexpr u32 L_RR = L_RA + 4096 + 128;
constexpr u32 L_RQ = L_RR + 9 * 512;
constexpr u32 L_CP = L_RQ + 4 * 512;
constexpr u32 L_CS = L_CP + 8 * 128;
constexpr u32 L_ST = L_CS + 3 * 128;
constexpr u32 L_QB = L_ST + 128;
constexpr u32 L_ZERO = L_QB + 2048;
constexpr u32 L_END = L_ZERO + 16;
static_assert(L_END <= 160 * 1024 && L_END > 80 * 1024, "LDS");

constexpr u32 SENT = 0xFFFFFFFFu;
constexpr u32 SPIN_MAX = 1u << 16;

#define POLL_SLEEP 0
#define SLP_O 0
#define SLP_Q 0
#define SLP_S4 20
#define SLP_S5 50
#define SLP_X_NONE 90
#define SLP_X_S3 55
#define SLP_X_S4 20

struct Args {
  const u16* x; const u16* k; const u16* v; const u16* wq; const u16* wo;
  const u16* wg; const u16* wu; const u16* wd; u16* out; unsigned char* ws;
};

typedef __amdgpu_buffer_rsrc_t rsrc_t;
__device__ __forceinline__ rsrc_t mk_rsrc(const void* p, u32 bytes) { return __builtin_amdgcn_make_buffer_rsrc((void*)p, 0, (int)bytes, 0x00020000); }

__device__ __forceinline__ float bflo(u32 d) { return __builtin_bit_cast(float, d << 16); }
__device__ __forceinline__ float bfhi(u32 d) { return __builtin_bit_cast(float, d & 0xffff0000u); }
__device__ __forceinline__ u32 cvtpk(float lo, float hi) {
  typedef __bf16 bf16x2 __attribute__((ext_vector_type(2)));
  typedef float f32x2 __attribute__((ext_vector_type(2)));
  f32x2 v = {lo, hi};
  return __builtin_bit_cast(u32, __builtin_convertvector(v, bf16x2));
}
__device__ __forceinline__ u32 nosent(u32 d) { return d < 0xFFFFFFFEu ? d : 0xFFFFFFFEu; }
template <int CTRL> __device__ __forceinline__ float dpp(float x) {
  return __builtin_bit_cast(float, __builtin_amdgcn_mov_dpp(__builtin_bit_cast(int, x), CTRL, 0xf, 0xf, true));
}
template <int CTRL> __device__ __forceinline__ u32 dppu(u32 x) { return (u32)__builtin_amdgcn_mov_dpp((int)x, CTRL, 0xf, 0xf, true); }
template <int RM, int BM> __device__ __forceinline__ float sel(float old, float src) {
  return __builtin_bit_cast(float, __builtin_amdgcn_update_dpp(__builtin_bit_cast(int, old), __builtin_bit_cast(int, src), 0xE4, RM, BM, false));
}
constexpr int ROR8 = 0x128, ROR4 = 0x124, ROR2 = 0x122, ROR1 = 0x121, SHL1 = 0x101, SHL4 = 0x104, SHL8 = 0x108, SHL12 = 0x10C;
__device__ __forceinline__ float wave_sum(float x) {
  x += dpp<ROR8>(x); x += dpp<ROR4>(x); x += dpp<ROR2>(x); x += dpp<ROR1>(x);
  auto s = __builtin_amdgcn_permlane16_swap(__float_as_uint(x), __float_as_uint(x), false, false);
  x = __uint_as_float(s[0]) + __uint_as_float(s[1]);
  auto t = __builtin_amdgcn_permlane32_swap(__float_as_uint(x), __float_as_uint(x), false, false);
  return __uint_as_float(t[0]) + __uint_as_float(t[1]);
}
__device__ __forceinline__ float wave_max(float x) {
  x = fmaxf(x, dpp<ROR8>(x)); x = fmaxf(x, dpp<ROR4>(x)); x = fmaxf(x, dpp<ROR2>(x)); x = fmaxf(x, dpp<ROR1>(x));
  auto s = __builtin_amdgcn_permlane16_swap(__float_as_uint(x), __float_as_uint(x), false, false);
  x = fmaxf(__uint_as_float(s[0]), __uint_as_float(s[1]));
  auto t = __builtin_amdgcn_permlane32_swap(__float_as_uint(x), __float_as_uint(x), false, false);
  return fmaxf(__uint_as_float(t[0]), __uint_as_float(t[1]));
}
__device__ __forceinline__ f4 mfma(v4u a, v4u b, f4 c) {
  return __builtin_amdgcn_mfma_f32_16x16x32_bf16(__builtin_bit_cast(bf16x8, a), __builtin_bit_cast(bf16x8, b), c, 0, 0, 0);
}
__device__ __forceinline__ u32 umax3(u32 a, u32 b, u32 c) { u32 m = a > b ? a : b; return m > c ? m : c; }
__device__ __forceinline__ u32 umax4(v4u v) { return umax3(v.x, v.y, v.z > v.w ? v.z : v.w); }
__device__ __forceinline__ bool all_valid(u32 mx) { return __builtin_amdgcn_ballot_w64(mx == SENT) == 0ull; }
__device__ __forceinline__ void lds_barrier() {
  asm volatile("s_waitcnt lgkmcnt(0)" ::: "memory");
  __builtin_amdgcn_s_barrier();
  asm volatile("" ::: "memory");
}
__device__ __forceinline__ v4u wfrag(LAS unsigned char* p) {
  typedef short v4i16_t __attribute__((ext_vector_type(4)));
  v4i16_t lo = __builtin_amdgcn_ds_read_tr16_b64_v4i16((LAS v4i16_t*)p);
  v4i16_t hi = __builtin_amdgcn_ds_read_tr16_b64_v4i16((LAS v4i16_t*)(p + 256));
  v2u a = __builtin_bit_cast(v2u, lo), b = __builtin_bit_cast(v2u, hi);
  return v4u{a.x, a.y, b.x, b.y};
}
#define WAITVM(n) asm volatile("s_waitcnt vmcnt(" #n ")" ::: "memory")
#define WAITVM_(n) WAITVM(n)
#define WAITVM_I(n) asm volatile("s_waitcnt vmcnt(%0)" ::"n"(n) : "memory")
__device__ __forceinline__ void dma16(u32 ldsaddr, u32 voff, rsrc_t rs, u32 soff) {
  asm volatile("s_mov_b32 m0, %0\n\ts_nop 0\n\tbuffer_load_dwordx4 %1, %2, %3 offen nt lds" ::"s"(ldsaddr), "v"(voff), "s"(rs), "s"(soff) : "memory");
}
__device__ __forceinline__ void st16_sc1(v4u d, u32 voff, rsrc_t rs) {
  asm volatile("buffer_store_dwordx4 %0, %1, %2, 0 offen sc1\n\ts_nop 1" ::"v"(d), "v"(voff), "s"(rs) : "memory");
}
__device__ __forceinline__ void st8_sc1(v2u d, u32 voff, rsrc_t rs) {
  asm volatile("buffer_store_dwordx2 %0, %1, %2, 0 offen sc1\n\ts_nop 1" ::"v"(d), "v"(voff), "s"(rs) : "memory");
}
__device__ __forceinline__ v4u quad_gather(u32 d) {
  return v4u{dppu<0x00>(d), dppu<0x55>(d), dppu<0xAA>(d), dppu<0xFF>(d)};
}
template <int N> __device__ __forceinline__ void idle() {
  if (N > 0) __builtin_amdgcn_s_sleep(N > 127 ? 127 : N);
}

#define LAUNDER_V(x) asm volatile("" : "+v"(x))
#define LAUNDER_S(x) asm volatile("" : "+s"(x))
typedef const __attribute__((address_space(4))) Args* kargs_t;
#define STAGE_VARS() int lane = lane_; LAUNDER_V(lane); const int g4 = lane >> 4, c16 = lane & 15; (void)g4; (void)c16; \
    int w = w_, c = c_; LAUNDER_S(w); LAUNDER_S(c); const int g = c & 7, r = c >> 3; (void)g; (void)r; \
    const int h = 4 * g + (r >> 3), bh = r & 7, b = bh >> 1, half = bh & 1; (void)h; (void)b; (void)half; \
    kargs_t ka = ka_; LAUNDER_S(ka); (void)ka

#define STAMP(i) do { } while (0)
#define STAMP1(i) do { } while (0)
#define STAMPA(i) do { } while (0)
#define STAMPB(i) do { } while (0)
#define STAMPVAL(i, val) do { } while (0)
#define CST(i) do { } while (0)
#define STAMP_OUT() do { } while (0)

constexpr u32 ONES = 0x3F803F80u;
__device__ __forceinline__ float dot2(u32 a, u32 b, float c) {
  typedef __bf16 bf16x2 __attribute__((ext_vector_type(2)));
  return __builtin_amdgcn_fdot2_f32_bf16(__builtin_bit_cast(bf16x2, a), __builtin_bit_cast(bf16x2, b), c, false);
}
__device__ __forceinline__ f2 row_stats(v4u xa, v4u xb) {
  float s1 = 0.f, s2 = 0.f, t1 = 0.f, t2 = 0.f;
#pragma unroll
  for (int j = 0; j < 4; ++j) { s1 = dot2(xa[j], ONES, s1); s2 = dot2(xa[j], xa[j], s2); t1 = dot2(xb[j], ONES, t1); t2 = dot2(xb[j], xb[j], t2); }
  const float mean = wave_sum(s1 + t1) * (1.f / DM);
  const float var = fmaxf(fmaf(-mean, mean, wave_sum(s2 + t2) * (1.f / DM)), 0.f);
  return f2{mean, __builtin_amdgcn_rsqf(var + 1e-5f)};
}
__device__ __forceinline__ u32 red_lane(int lane) { return ((lane >> 3) & 1) * 256 + (lane >> 4) * 64 + (lane & 7) * 8; }
template <int NP, int STRIDE> __device__ __forceinline__ f2 red_sum(LAS unsigned char* p) {
  f2 v[NP];
#pragma unroll
  for (int i = 0; i < NP; ++i) v[i] = *(LAS f2*)(p + i * STRIDE);
#pragma unroll
  for (int st = 1; st < NP; st *= 2)
#pragma unroll
    for (int i = 0; i + st < NP; i += 2 * st) v[i] += v[i + st];
  return v[0];
}
#define MF(d, a, b, c) "v_mfma_f32_16x16x32_bf16 " d ", " a ", " b ", " c "\n\t"
#define MV1(d, s, n) "v_mov_b32_dpp " d ", " s " row_shl:" n " row_mask:0xf bank_mask:0xf bound_ctrl:1\n\t"
#define MVT(s0, s1, s2, s3, n) MV1("v120", s0, n) MV1("v121", s1, n) MV1("v122", s2, n) MV1("v123", s3, n)
#define MVU(s0, s1, s2, s3, n) MV1("v124", s0, n) MV1("v125", s1, n) MV1("v126", s2, n) MV1("v127", s3, n)
#define A_TS(n) ""
#define TS_CLOBBER
#define TS_FLUSH(base) do { } while (0)
#define A_POLL ".Lp%=:\n\t"
#define A_DONE(t) "v_cmp_eq_u32 vcc, -1, " t "\n\ts_cbranch_vccz .Ld%=\n\ts_sub_u32 %[cnt], %[cnt], 1\n\ts_cbranch_scc0 .Lp%=\n.Ld%=:\n\t"
#define A_EXEC_C4 "s_mov_b32 exec_lo, 0x000f000f\n\ts_mov_b32 exec_hi, 0x000f000f\n\t"
#define A_EXEC_ALL "s_mov_b64 exec, -1\n\t"
#define A_BAR "s_waitcnt lgkmcnt(0)\n\ts_barrier"
#define A_PKADD(d, a, b) "v_pk_add_f32 " d ", " a ", " b "\n\t"
#define A_PACK_QUAD "v_cvt_pk_bf16_f32 v96, v96, v97\n\tv_min_u32 v96, -2, v96\n\ts_nop 1\n\t" \
      "v_mov_b32_dpp v100, v96 quad_perm:[0,0,0,0] row_mask:0xf bank_mask:0xf\n\tv_mov_b32_dpp v101, v96 quad_perm:[1,1,1,1] row_mask:0xf bank_mask:0xf\n\t" \
      "v_mov_b32_dpp v102, v96 quad_perm:[2,2,2,2] row_mask:0xf bank_mask:0xf\n\tv_mov_b32_dpp v103, v96 quad_perm:[3,3,3,3] row_mask:0xf bank_mask:0xf"
#define A_SUM8 "ds_read2st64_b64 v[96:99], %[rl] offset0:0 offset1:1\n\tds_read2st64_b64 v[104:107], %[rl] offset0:2 offset1:3\n\t" \
      "ds_read2st64_b64 v[108:111], %[rl] offset0:4 offset1:5\n\tds_read2st64_b64 v[112:115], %[rl] offset0:6 offset1:7\n\t" \
      "s_waitcnt lgkmcnt(3)\n\t" A_PKADD("v[96:97]", "v[96:97]", "v[98:99]") A_PKADD("v[96:97]", "v[96:97]", "%[res]") \
      "s_waitcnt lgkmcnt(2)\n\t" A_PKADD("v[104:105]", "v[104:105]", "v[106:107]") \
      "s_waitcnt lgkmcnt(1)\n\t" A_PKADD("v[108:109]", "v[108:109]", "v[110:111]") A_PKADD("v[96:97]", "v[96:97]", "v[104:105]") \
      "s_waitcnt lgkmcnt(0)\n\t" A_PKADD("v[112:113]", "v[112:113]", "v[114:115]") A_PKADD("v[96:97]", "v[96:97]", "v[108:109]") \
      A_PKADD("v[96:97]", "v[96:97]", "v[112:113]")
#define DS1(x) "v_dot2c_f32_bf16 %[s1], %[one], " x "\n\tv_dot2c_f32_bf16 %[s2], " x ", " x "\n\t"
#define RED2(ctl) "v_add_f32_dpp %[s1], %[s1], %[s1] " ctl " row_mask:0xf bank_mask:0xf\n\tv_add_f32_dpp %[s2], %[s2], %[s2] " ctl " row_mask:0xf bank_mask:0xf\n\t"
#define SWP2(op) "v_mov_b32 %[t1], %[s1]\n\tv_mov_b32 %[t2], %[s2]\n\ts_nop 1\n\t" op " %[s1], %[t1]\n\t" op " %[s2], %[t2]\n\tv_add_f32 %[s1], %[s1], %[t1]\n\tv_add_f32 %[s2], %[s2], %[t2]\n\t"
#define Z32_CLOBBER "v96", "v97", "v98", "v99", "v100", "v101", "v102", "v103", "v104", "v105", "v106", "v107", "v108", "v109", "v110", "v111", \
                    "v112", "v113", "v114", "v115", "v116", "v117", "v118", "v119", TU_CLOBBER
#define Z28_CLOBBER "v96", "v97", "v98", "v99", "v104", "v105", "v106", "v107", "v108", "v109", "v110", "v111", \
                    "v112", "v113", "v114", "v115", "v116", "v117", "v118", "v119", TU_CLOBBER
#define RT "v[120:123]"
#define RU "v[124:127]"
#define TU_CLOBBER "v120", "v121", "v122", "v123", "v124", "v125", "v126", "v127"

__global__ void __launch_bounds__(NT, 2) fused_decode(Args a_) {
  extern __shared__ __attribute__((aligned(16))) unsigned char smem_[];
  LAS unsigned char* const L = (LAS unsigned char*)smem_;
  const int lane_ = threadIdx.x & 63;
  const int w_ = __builtin_amdgcn_readfirstlane(threadIdx.x >> 6);
  const int c_ = blockIdx.x;
  const kargs_t ka_ = (kargs_t)__builtin_amdgcn_kernarg_segment_ptr();
  const int role = ((c_ & 7) < 4 && (c_ >> 3) < 20) ? 3 : (((c_ & 7) == 4 || (c_ & 7) == 5) && (c_ >> 3) < 27) ? 4 : ((c_ & 7) == 6 && (c_ >> 3) < 20) ? 5 : 0;
  const rsrc_t rs_ws = mk_rsrc(a_.ws, WS_BYTES);
  u16* const outp = a_.out;
  STAMP(11);

  v4u kr[16], vr[16];
  v4u xa, xb;
  u32 tag_lo, tag_hi;
  rsrc_t rs_wr;
  u32 wr_lane, wr_step, wr_lds;
#define ROLE_DMA(i) dma16(wr_lds + (i) * 1024, wr_lane, rs_wr, (i) * wr_step)
#define NE3 6
#define NE 4
#define EARLY_DMA(i) do { if (it == 0 && ((i) < NE ? role != 0 : (i) < NE3 ? role == 3 : false)) ROLE_DMA(i); } while (0)
#define WAIT_EARLY(base, n) do { if (role == 0) WAITVM_I(base); else if (role == 3) WAITVM_I((base) + (NE3 < (n) ? NE3 : (n))); else WAITVM_I((base) + (NE < (n) ? NE : (n))); } while (0)
#define KV_DESC() const size_t kvoff = ((size_t)(b * 32 + h) * SK + (SK - WIN) + half * 512 + w * 64) * DH; \
  const rsrc_t rs_k = mk_rsrc(ka->k + kvoff, 16384), rs_v = mk_rsrc(ka->v + kvoff, 16384); (void)rs_v; \
  const u32 voK0 = (lane & 7) * 1024 + ((lane >> 3) & 1) * 64 + g4 * 16, voK1 = voK0 + 8192, voV = lane * 16; (void)voV
#define LDK(i) asm volatile("buffer_load_dwordx4 %0, %1, %2, 0 offen offset:%3 nt" : "+a"(kr[i]) : "v"((((i) >> 1) & 1) ? voK1 : voK0), "s"(rs_k), "n"(((i) >> 2) * 256 + ((i) & 1) * 128) : "memory")
#define LDV(i) asm volatile("buffer_load_dwordx4 %0, %1, %2, %3 offen nt" : "+v"(vr[i]) : "v"(voV), "s"(rs_v), "s"((u32)((i) * 1024)) : "memory")
#define DMA_LANE(stride) ((u32)(lane >> 2) * (stride) + (u32)(((lane & 3) ^ (((lane >> 5) & 1) << 1)) * 16))
#define WF0() ((u32)((8 * g4 + (c16 >> 2)) * 64 + ((((c16 & 3) >> 1) ^ ((g4 & 1) << 1)) * 16) + (c16 & 1) * 8))
#define QS0(w) ((w) < 6 ? 3 * ((w) - 4) : 2 * (w) - 6)

  {
    STAGE_VARS();
    KV_DESC();
#pragma unroll
    for (int i = 0; i < 16; ++i) { asm volatile("" : "=a"(kr[i])); asm volatile("" : "=v"(vr[i])); }
    asm volatile("s_nop 4" ::: "memory");
    LDK(0); LDK(1);
    STAMP(0);
    if (w == 3 && lane == 0) *(LAS v4u*)(L + L_ZERO) = v4u{0, 0, 0, 0};
    const v4u sv = {SENT, SENT, SENT, SENT};
    if (w < 2) st16_sc1(sv, WS_S + c * 2048 + (w * 64 + lane) * 16, rs_ws);
    v2u gen;
    asm volatile("buffer_load_dwordx2 %0, %1, %2, 0 offen sc1" : "=v"(gen) : "v"(WS_G), "s"(rs_ws) : "memory");
    const rsrc_t rs_x = mk_rsrc(ka->x, 4 * DM * 2);
    const int slot = c16 >> 2, qs0 = QS0(w);
    const u32 o1 = w < 4 ? w * 1280 + lane * 16 : slot < (w < 6 ? 3 : 2) ? (c16 & 3) * 1280 + ((bh & 1) * 320 + (qs0 + slot) * 32 + 8 * g4) * 2 : OOB;
    const u32 o2 = (w < 4 && lane < 16) ? w * 1280 + 1024 + lane * 16 : OOB;
    asm volatile("s_nop 4\n\tbuffer_load_dwordx4 %0, %2, %4, 0 offen\n\tbuffer_load_dwordx4 %1, %3, %4, 0 offen"
                 : "=&v"(xa), "=&v"(xb) : "v"(o1), "v"(o2), "s"(rs_x) : "memory");
    if (w >= 4) {
      const rsrc_t rs = mk_rsrc(ka->wq + (size_t)(320 * (bh & 1) + 32 * qs0) * 4096 + h * 128 + 32 * (bh >> 1), 96 * 8192);
      const u32 vo = DMA_LANE(8192);
      asm volatile("s_nop 4" ::: "memory");
#pragma unroll
      for (int i = 0; i < 4; ++i) dma16(L_WQ + qs0 * 2048 + i * 1024, vo, rs, i * 16 * 8192);
      if (w < 6) { dma16(L_WQ + qs0 * 2048 + 4 * 1024, vo, rs, 4 * 16 * 8192); dma16(L_WQ + qs0 * 2048 + 5 * 1024, vo, rs, 5 * 16 * 8192); }
    }
    {
      const int s0 = w < 6 ? 7 * w : 42 + 6 * (w - 6), j4 = (g - 4) * 27 + r;
      const u16* base = role == 3 ? ka->wo + (size_t)(1024 * g + 128 * w) * DM + 32 * r
                      : role == 4 ? ((w & 1) ? ka->wu : ka->wg) + (size_t)(160 * (w >> 1)) * FF + 32 * j4
                                  : ka->wd + (size_t)(32 * s0) * DM + 32 * (r < 20 ? r : 0);
      const u32 stride = role == 4 ? 3456 : 1280;
      const u32 rows = role == 3 ? 128 : role == 4 ? 160 : role == 5 ? (w < 6 ? 224 : 192) : 0;
      rs_wr = mk_rsrc(base, rows * stride);
      wr_lane = DMA_LANE(stride);
      wr_step = 16 * stride;
      wr_lds = role == 3 ? L_WR + w * 8192 : role == 4 ? L_WR + (w & 1) * 40960 + (w >> 1) * 10240 : role == 5 ? L_WR + s0 * 2048 : L_WR + w * 8192;
    }
    __builtin_amdgcn_s_barrier();
    LDK(2); LDK(3); LDK(4); LDK(5); LDK(6); LDK(7); LDK(8); LDK(9);
    WAITVM(8);
    asm volatile("" : "+v"(gen), "+v"(xa), "+v"(xb));
    const u64 tag64 = (((u64)(u32)__builtin_amdgcn_readfirstlane(gen.y) << 32) | (u32)__builtin_amdgcn_readfirstlane(gen.x)) + 1ull;
    tag_lo = (u32)tag64; tag_hi = (u32)(tag64 >> 32);
    __builtin_amdgcn_s_barrier();
    if (w == 3) st8_sc1(v2u{tag_lo, tag_hi}, lane == 0 ? WS_FLAG + c * 8 : OOB, rs_ws);
  }

#pragma unroll 1
  for (int it = 0; it < 2; ++it) {
    {
      STAGE_VARS();
      const int qs0 = QS0(w);
      v4u wqf[6];
      if (w >= 4) {
        const u32 wf0 = WF0();
#pragma unroll
        for (int s = 0; s < 3; ++s) {
          const int sl = (s == 2 && w >= 6) ? qs0 : qs0 + s;
#pragma unroll
          for (int t = 0; t < 2; ++t) wqf[s * 2 + t] = wfrag(L + L_WQ + sl * 2048 + (wf0 ^ (t * 32)));
        }
#pragma unroll
        for (int i = 0; i < 6; ++i) asm volatile("" : "+a"(wqf[i]));
      } else {
#pragma unroll
        for (int i = 0; i < 6; ++i) asm volatile("" : "=a"(wqf[i]));
      }
      u32 rl = L_RQ + red_lane(lane), str = L_ST + g4 * 8;
      u32 qout = it == 0 ? ((lane & 1) ? OOB : WS_Q1 + c * 512 + (lane >> 1) * 16) : ((lane & 3) ? OOB : S_Q2 + (h * 8 + bh) * 256 + lane * 4);
      u32 lw = w < 4 ? L_XR + w * 1280 + lane * 16 : L_RQ + (w - 4) * 512 + c16 * 64 + g4 * 16;
      u32 stw = L_ST + (w & 3) * 8;
      u32 o1 = OOB, o2 = OOB;
      f2 cs = {0.f, 0.f};
      if (it == 0) {
        if (w >= 4) {
          const v4u one = {ONES, ONES, ONES, ONES};
          const u32 z = w < 6 ? ONES : 0u;
          const v4u one3 = {z, z, z, z};
          f4 c0, c1;
          asm volatile("s_nop 1\n\t" MF("%0", "%2", "%8", "0") MF("%1", "%3", "%8", "0") MF("%0", "%4", "%8", "%0") MF("%1", "%5", "%8", "%1")
                       MF("%0", "%6", "%9", "%0") MF("%1", "%7", "%9", "%1") "s_nop 7"
                       : "=&v"(c0), "=&v"(c1)
                       : "a"(wqf[0]), "a"(wqf[1]), "a"(wqf[2]), "a"(wqf[3]), "a"(wqf[4]), "a"(wqf[5]), "v"(one), "v"(one3));
          if (c16 == 0) {
            *(LAS f4*)(L + L_CP + (w - 4) * 128 + g4 * 16) = c0;
            *(LAS f4*)(L + L_CP + (w - 4) * 128 + 64 + g4 * 16) = c1;
          }
        }
      } else {
        cs = *(LAS f2*)(L + L_CS + c16 * 8);
        const u32 base = S_X2 + g * 5120;
        const int slot = c16 >> 2;
        o1 = w < 4 ? base + w * 64 + (lane >> 2) * 256 + (lane & 3) * 16
                   : slot < (w < 6 ? 3 : 2) ? base + ((bh & 1) * 10 + qs0 + slot) * 256 + (c16 & 3) * 64 + g4 * 16 : OOB;
        o2 = (w < 4 && lane < 16) ? base + w * 64 + (16 + (lane >> 2)) * 256 + (lane & 3) * 16 : OOB;
        if (role == 0) idle<SLP_X_NONE>(); else if (role == 3) idle<SLP_X_S3>(); else if (role == 4) idle<SLP_X_S4>();
      }
      LAUNDER_V(rl); LAUNDER_V(str); LAUNDER_V(qout); LAUNDER_V(lw); LAUNDER_V(stw); LAUNDER_V(o1); LAUNDER_V(o2); LAUNDER_V(cs);
      STAMP1(32);
      u32 cnt = SPIN_MAX;
      if (w < 4) {
        float s1, s2, t1, t2;
        asm volatile(A_TS(0) "s_cmp_eq_u32 %[it], 0\n\ts_cbranch_scc1 .Ld%=\n" A_POLL
            "buffer_load_dwordx4 v[96:99], %[o1], %[rs], 0 offen sc1\n\tbuffer_load_dwordx4 v[100:103], %[o2], %[rs], 0 offen sc1\n\ts_waitcnt vmcnt(0)\n\t"
            "v_max3_u32 v104, v96, v97, v98\n\tv_max3_u32 v105, v99, v100, v101\n\tv_max3_u32 v104, v104, v102, v103\n\tv_max_u32 v104, v104, v105\n\t"
            A_DONE("v104") A_TS(1)
            "v_mov_b32 %[s1], 0\n\tv_mov_b32 %[s2], 0\n\tds_write_b128 %[lw], v[96:99]\n\t"
            DS1("v96") DS1("v97") DS1("v98") DS1("v99") DS1("v100") DS1("v101") DS1("v102") DS1("v103")
            "s_mov_b32 exec_lo, 0xffff\n\ts_mov_b32 exec_hi, 0\n\tds_write_b128 %[lw], v[100:103] offset:1024\n\ts_mov_b64 exec, -1\n\t"
            RED2("row_ror:8") "s_nop 0\n\t" RED2("row_ror:4") "s_nop 0\n\t" RED2("row_ror:2") "s_nop 0\n\t" RED2("row_ror:1")
            SWP2("v_permlane16_swap_b32") SWP2("v_permlane32_swap_b32")
            "v_mul_f32 v104, 0x3acccccd, %[s1]\n\tv_mul_f32 v105, 0x3acccccd, %[s2]\n\tv_fma_f32 v105, -v104, v104, v105\n\tv_max_f32 v105, 0, v105\n\t"
            "v_add_f32 v105, 0x3727c5ac, v105\n\tv_rsq_f32 v105, v105\n\t"
            A_TS(2) "s_mov_b64 exec, 1\n\tds_write_b64 %[stw], v[104:105]\n\ts_mov_b64 exec, -1\n\t" A_BAR
            : "+{v[96:99]}"(xa), "+{v[100:103]}"(xb), [cnt] "+s"(cnt), [s1] "=&v"(s1), [s2] "=&v"(s2), [t1] "=&v"(t1), [t2] "=&v"(t2)
            : [o1] "v"(o1), [o2] "v"(o2), [rs] "s"(rs_ws), [it] "s"(it), [lw] "v"(lw), [stw] "v"(stw), [one] "s"(ONES)
            : "v104", "v105", "vcc", "scc", "memory" TS_CLOBBER);
        TS_FLUSH(48);
      } else {
        asm volatile(A_TS(0) "s_nop 1\n\ts_cmp_eq_u32 %[it], 0\n\ts_cbranch_scc1 .Ld%=\n" A_POLL
            "buffer_load_dwordx4 v[96:99], %[o1], %[rs], 0 offen sc1\n\ts_waitcnt vmcnt(0)\n\t"
            "v_max3_u32 v104, v96, v97, v98\n\tv_max_u32 v104, v104, v99\n\t"
            A_DONE("v104") A_TS(1)
            MF("v[104:107]", "%[w0]", "v[96:99]", "0") MVT("v96", "v97", "v98", "v99", "4") MF("v[108:111]", "%[w1]", "v[96:99]", "0") MVU("v96", "v97", "v98", "v99", "8")
            MF("v[104:107]", "%[w2]", RT, "v[104:107]") MF("v[108:111]", "%[w3]", RT, "v[108:111]")
            MF("v[104:107]", "%[w4]", RU, "v[104:107]") MF("v[108:111]", "%[w5]", RU, "v[108:111]")
            "s_nop 5\n\t" A_TS(2) A_EXEC_C4 "ds_write_b128 %[lw], v[104:107]\n\tds_write_b128 %[lw], v[108:111] offset:256\n\t" A_EXEC_ALL A_BAR
            : "+{v[96:99]}"(xa), [cnt] "+s"(cnt)
            : [o1] "v"(o1), [rs] "s"(rs_ws), [it] "s"(it), [lw] "v"(lw),
              [w0] "a"(wqf[0]), [w1] "a"(wqf[1]), [w2] "a"(wqf[2]), [w3] "a"(wqf[3]), [w4] "a"(wqf[4]), [w5] "a"(wqf[5])
            : "v104", "v105", "v106", "v107", "v108", "v109", "v110", "v111", TU_CLOBBER, "vcc", "scc", "memory" TS_CLOBBER);
        TS_FLUSH(48);
      }
      xa = v4u{0, 0, 0, 0}; xb = v4u{0, 0, 0, 0};
      STAMP(1 + it * 12);
      if (w == 0) {
        if (it == 0) { cs = red_sum<4, 128>(L + L_CP + c16 * 8); if (lane < 16) *(LAS f2*)(L + L_CS + c16 * 8) = cs; }
        u32 d;
        asm volatile(
            "ds_read2st64_b64 v[96:99], %[rl] offset0:0 offset1:1\n\tds_read2st64_b64 v[104:107], %[rl] offset0:2 offset1:3\n\tds_read_b64 v[108:109], %[str]\n\t"
            "s_waitcnt lgkmcnt(2)\n\t" A_PKADD("v[96:97]", "v[96:97]", "v[98:99]")
            "s_waitcnt lgkmcnt(1)\n\t" A_PKADD("v[104:105]", "v[104:105]", "v[106:107]")
            "s_waitcnt lgkmcnt(0)\n\t" A_PKADD("v[96:97]", "v[96:97]", "v[104:105]")
            "v_pk_fma_f32 v[96:97], %[cs], v[108:109], v[96:97] op_sel_hi:[1,0,1] neg_lo:[0,1,0] neg_hi:[0,1,0]\n\t"
            "v_pk_mul_f32 v[96:97], v[96:97], v[108:109] op_sel:[0,1] op_sel_hi:[1,1]\n\t"
            "v_cvt_pk_bf16_f32 %[d], v96, v97\n\tv_min_u32 %[d], -2, %[d]\n\ts_nop 1"
            : [d] "=&v"(d) : [rl] "v"(rl), [str] "v"(str), [cs] "v"(cs)
            : "v96", "v97", "v98", "v99", "v104", "v105", "v106", "v107", "v108", "v109", "memory");
        if (it == 0) st16_sc1(v4u{d, dppu<SHL1>(d), tag_lo, tag_hi}, qout, rs_ws);
        else st16_sc1(quad_gather(d), qout, rs_ws);
      }
      STAMP(2 + it * 12);
    }
    v2u qv;
    if (it == 0) {
      STAGE_VARS();
      KV_DESC();
      asm volatile("s_nop 4" ::: "memory");
      const u32 q1off = WS_Q1 + (g + 8 * ((h & 3) * 8 + (lane >> 3))) * 512 + b * 128 + (lane & 7) * 16;
      v4u p1, fl0, fl1;
#define W3 4
#define WN 3
#define THR() WAITVM_(WN)
#define KD(i) THR(); LDK(8 + i);
      THR(); LDK(10); THR(); LDK(11); THR(); LDK(12); THR(); LDK(13); THR(); LDK(14); THR(); LDK(15);
      THR(); LDV(0); THR(); LDV(1); THR(); LDV(2); THR(); LDV(3);
      asm volatile("buffer_load_dwordx4 %0, %1, %2, 0 offen sc1" : "=v"(p1) : "v"(q1off), "s"(rs_ws) : "memory");
      THR(); LDV(4); THR(); LDV(5); THR(); LDV(6); THR(); LDV(7);
      asm volatile("buffer_load_dwordx4 %0, %2, %3, 0 offen sc1\n\tbuffer_load_dwordx4 %1, %2, %3, 0 offen offset:1024 sc1"
                   : "=&v"(fl0), "=&v"(fl1) : "v"(w == 7 ? WS_FLAG + lane * 16 : OOB), "s"(rs_ws) : "memory");
#define WT 3
#define THT() WAITVM_(WT)
      THT(); LDV(8); THT(); LDV(9); THT(); LDV(10); THT(); LDV(11); THT(); LDV(12); THT(); LDV(13); THT(); LDV(14);
      THT(); LDV(15);
      if (WT > 3) {   }
      if (WT > 3) WAITVM(4);
      EARLY_DMA(0);
      STAMP(3);
      asm volatile("" : "+v"(p1), "+v"(fl0), "+v"(fl1));
      _Pragma("nounroll") for (u32 spin = 0; spin < SPIN_MAX; ++spin) {
        if (__builtin_amdgcn_ballot_w64(p1.z != tag_lo || p1.w != tag_hi) == 0ull) break;
        asm volatile("buffer_load_dwordx4 %0, %1, %2, 0 offen sc1\n\ts_waitcnt vmcnt(0)" : "=&v"(p1) : "v"(q1off), "s"(rs_ws) : "memory");
      }
      qv = v2u{p1.x, p1.y};
      if (w == 7) {
        _Pragma("nounroll") for (u32 spin = 0; spin < SPIN_MAX; ++spin) {
          const bool bad = fl0.x != tag_lo || fl0.y != tag_hi || fl0.z != tag_lo || fl0.w != tag_hi || fl1.x != tag_lo || fl1.y != tag_hi || fl1.z != tag_lo || fl1.w != tag_hi;
          if (__builtin_amdgcn_ballot_w64(bad) == 0ull) break;
          asm volatile("buffer_load_dwordx4 %0, %2, %3, 0 offen sc1\n\tbuffer_load_dwordx4 %1, %2, %3, 0 offen offset:1024 sc1\n\ts_waitcnt vmcnt(0)"
                       : "=&v"(fl0), "=&v"(fl1) : "v"(WS_FLAG + lane * 16), "s"(rs_ws) : "memory");
        }
      }
    } else {
      qv = v2u{0, 0};
    }
    const u32 pbase = it * PASS_STRIDE;
    {
      STAGE_VARS();
      u32 qoff = S_Q2 + ((h * 8 + (lane >> 3)) * 4 + b) * 64 + (lane & 7) * 8;
      u32 qbw = L_QB + w * 256 + g4 * 64 + (lane & 7) * 8, qbr = L_QB + w * 256 + (c16 & 1) * 64 + g4 * 16;
      u32 raw = L_RA + w * 512 + c16 * 32 + g4 * 8, rsw = L_RA + 4096 + w * 8;
      u32 rar = L_RA + lane * 8, rsr = L_RA + 4096 + (lane & 7) * 8;
      const u32 oslot = S_O0 + pbase + ((h * 2 + half) * 4 + b) * O_SLOT;
      u32 oout = (lane & 3) ? OOB : oslot + lane * 4, sout = lane == 0 ? oslot + 256 : OOB;
      LAUNDER_V(qoff); LAUNDER_V(qbw); LAUNDER_V(qbr); LAUNDER_V(raw); LAUNDER_V(rsw); LAUNDER_V(rar); LAUNDER_V(rsr); LAUNDER_V(oout); LAUNDER_V(sout);
      if (w < 4) __builtin_amdgcn_s_setprio(1);
      if (it == 1) idle<SLP_Q>();
      STAMP1(33);
      float sraw;
      {
        float sb;
        u32 cnt = SPIN_MAX;
#define SEL(d, s, rm, bm) "v_mov_b32_dpp " d ", " s " quad_perm:[0,1,2,3] row_mask:" rm " bank_mask:" bm "\n\t"
#define QKMF0(d, k) "v_mfma_f32_16x16x32_bf16 " d ", v[88:91], %[" k "], 0\n\t"
#define QKMF1(d, k) "v_mfma_f32_16x16x32_bf16 " d ", v[92:95], %[" k "], " d "\n\t"
        asm volatile(A_TS(0) "s_cmp_eq_u32 %[it], 0\n\ts_cbranch_scc1 .Ld%=\n" A_POLL
            "buffer_load_dwordx2 v[96:97], %[qo], %[rs], 0 offen sc1\n\ts_waitcnt vmcnt(0)\n\tv_max_u32 v98, v96, v97\n\t"
            A_DONE("v98") A_TS(1)
            "v_lshlrev_b32 v98, 16, v96\n\tv_and_b32 v99, 0xffff0000, v96\n\tv_lshlrev_b32 v100, 16, v97\n\tv_and_b32 v101, 0xffff0000, v97\n\t"
            "v_add_f32_dpp v98, v98, v98 row_ror:8 row_mask:0xf bank_mask:0xf\n\tv_add_f32_dpp v99, v99, v99 row_ror:8 row_mask:0xf bank_mask:0xf\n\t"
            "v_add_f32_dpp v100, v100, v100 row_ror:8 row_mask:0xf bank_mask:0xf\n\tv_add_f32_dpp v101, v101, v101 row_ror:8 row_mask:0xf bank_mask:0xf\n\t"
            "v_cvt_pk_bf16_f32 v96, v98, v99\n\tv_cvt_pk_bf16_f32 v97, v100, v101\n\tds_write_b64 %[qbw], v[96:97]\n\ts_waitcnt lgkmcnt(0)\n\t"
            "ds_read_b128 v[88:91], %[qbr]\n\tds_read_b128 v[92:95], %[qbr] offset:128\n\ts_waitcnt lgkmcnt(1)\n\t" A_TS(2)
            QKMF0("v[96:99]", "k0") QKMF0("v[100:103]", "k2") QKMF0("v[104:107]", "k4") QKMF0("v[108:111]", "k6")
            QKMF0("v[112:115]", "k8") QKMF0("v[116:119]", "k10") QKMF0("v[120:123]", "k12") QKMF0("v[124:127]", "k14")
            "s_waitcnt lgkmcnt(0)\n\t"
            QKMF1("v[96:99]", "k1") QKMF1("v[100:103]", "k3") QKMF1("v[104:107]", "k5") QKMF1("v[108:111]", "k7")
            QKMF1("v[112:115]", "k9") QKMF1("v[116:119]", "k11") QKMF1("v[120:123]", "k13") QKMF1("v[124:127]", "k15")
            "s_nop 0\n\tv_mov_b32 %[sa], v96\n\tv_mov_b32 %[sb], v100\n\t"
            SEL("%[sa]", "v101", "0x1", "0xc") SEL("%[sb]", "v97", "0x1", "0xc")
            SEL("%[sa]", "v104", "0x2", "0x3") SEL("%[sb]", "v108", "0x2", "0x3") SEL("%[sa]", "v109", "0x2", "0xc") SEL("%[sb]", "v105", "0x2", "0xc")
            SEL("%[sa]", "v112", "0x4", "0x3") SEL("%[sb]", "v116", "0x4", "0x3") SEL("%[sa]", "v117", "0x4", "0xc") SEL("%[sb]", "v113", "0x4", "0xc")
            SEL("%[sa]", "v120", "0x8", "0x3") SEL("%[sb]", "v124", "0x8", "0x3") SEL("%[sa]", "v125", "0x8", "0xc") SEL("%[sb]", "v121", "0x8", "0xc")
            "s_nop 1\n\tv_add_f32_dpp %[sa], %[sb], %[sa] row_ror:8 row_mask:0xf bank_mask:0xf"
            : "+{v[96:97]}"(qv), [sa] "=&v"(sraw), [sb] "=&v"(sb), [cnt] "+s"(cnt)
            : [qo] "v"(qoff), [rs] "s"(rs_ws), [it] "s"(it), [qbw] "v"(qbw), [qbr] "v"(qbr),
              [k0] "a"(kr[0]), [k1] "a"(kr[1]), [k2] "a"(kr[2]), [k3] "a"(kr[3]), [k4] "a"(kr[4]), [k5] "a"(kr[5]), [k6] "a"(kr[6]), [k7] "a"(kr[7]),
              [k8] "a"(kr[8]), [k9] "a"(kr[9]), [k10] "a"(kr[10]), [k11] "a"(kr[11]), [k12] "a"(kr[12]), [k13] "a"(kr[13]), [k14] "a"(kr[14]), [k15] "a"(kr[15])
            : "v88", "v89", "v90", "v91", "v92", "v93", "v94", "v95", "v98", "v99", "v100", "v101", "v102", "v103", "v104", "v105", "v106", "v107",
              "v108", "v109", "v110", "v111", "v112", "v113", "v114", "v115", "v116", "v117", "v118", "v119", TU_CLOBBER, "vcc", "scc", "memory" TS_CLOBBER);
        TS_FLUSH(52);
      }
      STAMP(4 + it * 12);
      EARLY_DMA(1);
      float mw, lw, oc[8];
      u32 pp;
      {
        float t;
#define RMAX(ctl) "v_max_f32_dpp %[m], %[m], %[m] " ctl " row_mask:0xf bank_mask:0xf\n\t"
        asm volatile(
            "v_mul_f32 %[l], 0x3e0293ee, %[s]\n\tv_mov_b32 %[o0], 0\n\tv_mov_b32 %[o1], 0\n\t"
            "v_max_f32_dpp %[m], %[l], %[l] row_ror:8 row_mask:0xf bank_mask:0xf\n\tv_mov_b32 %[o2], 0\n\tv_mov_b32 %[o3], 0\n\t"
            RMAX("row_ror:4") "v_mov_b32 %[o4], 0\n\tv_mov_b32 %[o5], 0\n\t" RMAX("row_ror:2") "v_mov_b32 %[o6], 0\n\tv_mov_b32 %[o7], 0\n\t" RMAX("row_ror:1")
            "v_mov_b32 %[t], %[m]\n\ts_nop 1\n\tv_permlane16_swap_b32 %[m], %[t]\n\tv_max_f32 %[m], %[m], %[t]\n\t"
            "v_mov_b32 %[t], %[m]\n\ts_nop 1\n\tv_permlane32_swap_b32 %[m], %[t]\n\tv_max_f32 %[m], %[m], %[t]\n\t"
            "v_sub_f32 %[l], %[l], %[m]\n\tv_exp_f32 %[l], %[l]\n\ts_nop 1\n\t"
            "v_mov_b32_dpp %[t], %[l] row_shl:1 row_mask:0xf bank_mask:0xf bound_ctrl:1\n\tv_cvt_pk_bf16_f32 %[p], %[l], %[t]"
            : [m] "=&v"(mw), [l] "=&v"(lw), [p] "=&v"(pp), [t] "=&v"(t), [o0] "=&v"(oc[0]), [o1] "=&v"(oc[1]), [o2] "=&v"(oc[2]), [o3] "=&v"(oc[3]),
              [o4] "=&v"(oc[4]), [o5] "=&v"(oc[5]), [o6] "=&v"(oc[6]), [o7] "=&v"(oc[7])
            : [s] "v"(sraw));
      }
      CST(56);
      EARLY_DMA(2);
#define VP1(a, b) "v_perm_b32 %8, " b ", " a ", %9\n\tv_perm_b32 " b ", " b ", " a ", %10\n\tv_mov_b32 " a ", %8\n\t"
#define VPAIR(m) do { u32 t_; asm volatile(VP1("%0", "%4") VP1("%1", "%5") VP1("%2", "%6") VP1("%3", "%7") \
      : "+v"(vr[2 * (m)][0]), "+v"(vr[2 * (m)][1]), "+v"(vr[2 * (m)][2]), "+v"(vr[2 * (m)][3]), \
        "+v"(vr[2 * (m) + 1][0]), "+v"(vr[2 * (m) + 1][1]), "+v"(vr[2 * (m) + 1][2]), "+v"(vr[2 * (m) + 1][3]), "=&v"(t_) \
      : "s"(0x05040100u), "s"(0x07060302u)); } while (0)
#define D2(d, v, bc) "v_dot2c_f32_bf16_dpp " d ", %10, " v " row_newbcast:" #bc " row_mask:0xf bank_mask:0xf\n\t"
#define LSUM(ctl) "v_add_f32_dpp %8, %8, %8 " ctl " row_mask:0xf bank_mask:0xf"
#define LSWP(op) "v_mov_b32 %9, %8\n\ts_nop 1\n\t" op " %8, %9\n\tv_add_f32 %8, %8, %9"
#define PV(m, bc, pre, post) do { float t_; asm volatile(pre D2("%0", "%11", bc) D2("%1", "%15", bc) D2("%2", "%12", bc) D2("%3", "%16", bc) \
      D2("%4", "%13", bc) D2("%5", "%17", bc) D2("%6", "%14", bc) D2("%7", "%18", bc) post \
      : "+v"(oc[0]), "+v"(oc[1]), "+v"(oc[2]), "+v"(oc[3]), "+v"(oc[4]), "+v"(oc[5]), "+v"(oc[6]), "+v"(oc[7]), "+v"(lw), "=&v"(t_) \
      : "v"(pp), "v"(vr[2 * (m)][0]), "v"(vr[2 * (m)][1]), "v"(vr[2 * (m)][2]), "v"(vr[2 * (m)][3]), \
        "v"(vr[2 * (m) + 1][0]), "v"(vr[2 * (m) + 1][1]), "v"(vr[2 * (m) + 1][2]), "v"(vr[2 * (m) + 1][3])); } while (0)
      if (it == 0) {
#pragma unroll
        for (int i = 0; i < 12; ++i) asm volatile("" : "+v"(vr[i]));
        VPAIR(0); VPAIR(1); VPAIR(2); VPAIR(3); VPAIR(4); VPAIR(5);
      }
      EARLY_DMA(3);
      PV(0, 0, "s_nop 1\n\t", LSUM("row_ror:8")); PV(1, 2, "", LSUM("row_ror:4"));
      EARLY_DMA(4);
      PV(2, 4, "", LSUM("row_ror:2")); PV(3, 6, "", LSUM("row_ror:1"));
      EARLY_DMA(5);
      PV(4, 8, "", LSWP("v_permlane16_swap_b32")); PV(5, 10, "", LSWP("v_permlane32_swap_b32"));
      EARLY_DMA(6);
      if (it == 0) {
        WAIT_EARLY(2, 7);
        asm volatile("" : "+v"(vr[12]), "+v"(vr[13]));
        VPAIR(6);
      }
      PV(6, 12, "", "");
      EARLY_DMA(7);
      if (it == 0) {
        WAIT_EARLY(0, 8);
        asm volatile("" : "+v"(vr[14]), "+v"(vr[15]));
        VPAIR(7);
      }
      PV(7, 14, "", "s_nop 2");
      STAMP(5 + it * 12);
      CST(57);
      asm volatile(
          "v_permlane32_swap_b32 %0, %4\n\tv_permlane32_swap_b32 %1, %5\n\tv_permlane32_swap_b32 %2, %6\n\tv_permlane32_swap_b32 %3, %7\n\t"
          "v_add_f32 %0, %0, %4\n\tv_add_f32 %2, %2, %6\n\tv_add_f32 %1, %1, %5\n\tv_add_f32 %3, %3, %7\n\t"
          "v_mov_b32 v98, %8\n\tv_permlane16_swap_b32 %0, %2\n\tv_mov_b32 v99, %9\n\tv_permlane16_swap_b32 %1, %3\n\t"
          "v_add_f32 v96, %0, %2\n\tv_add_f32 v97, %1, %3\n\tds_write_b64 %10, v[96:97]\n\t"
          "s_mov_b64 exec, 1\n\tds_write_b64 %11, v[98:99]\n\ts_mov_b64 exec, -1\n\ts_setprio 0\n\t" A_BAR
          : "+v"(oc[0]), "+v"(oc[1]), "+v"(oc[2]), "+v"(oc[3]), "+v"(oc[4]), "+v"(oc[5]), "+v"(oc[6]), "+v"(oc[7])
          : "v"(mw), "v"(lw), "v"(raw), "v"(rsw) : "v96", "v97", "v98", "v99", "memory");
      CST(58);
      if (w == 0) {
        v4u q;
        v2u st;
#define MRG(i, lo, hi) "v_fmac_f32_dpp v96, v120, " lo " row_newbcast:" #i " row_mask:0xf bank_mask:0xf\n\tv_fmac_f32_dpp v97, v120, " hi " row_newbcast:" #i " row_mask:0xf bank_mask:0xf\n\t"
        asm volatile(
            "ds_read_b64 v[118:119], %[rsr]\n\t"
            "ds_read2st64_b64 v[100:103], %[rar] offset0:0 offset1:1\n\tds_read2st64_b64 v[104:107], %[rar] offset0:2 offset1:3\n\t"
            "ds_read2st64_b64 v[108:111], %[rar] offset0:4 offset1:5\n\tds_read2st64_b64 v[112:115], %[rar] offset0:6 offset1:7\n\t"
            "v_mov_b32 v96, 0\n\tv_mov_b32 v97, 0\n\ts_waitcnt lgkmcnt(4)\n\t"
            "v_max_f32_dpp v116, v118, v118 quad_perm:[1,0,3,2] row_mask:0xf bank_mask:0xf\n\ts_nop 1\n\t"
            "v_max_f32_dpp v116, v116, v116 quad_perm:[2,3,0,1] row_mask:0xf bank_mask:0xf\n\ts_nop 1\n\t"
            "v_max_f32_dpp v116, v116, v116 row_half_mirror row_mask:0xf bank_mask:0xf\n\t"
            "v_sub_f32 v120, v118, v116\n\tv_exp_f32 v120, v120\n\ts_nop 0\n\tv_mul_f32 v117, v120, v119\n\ts_nop 1\n\t"
            "v_add_f32_dpp v117, v117, v117 quad_perm:[1,0,3,2] row_mask:0xf bank_mask:0xf\n\ts_nop 1\n\t"
            "v_add_f32_dpp v117, v117, v117 quad_perm:[2,3,0,1] row_mask:0xf bank_mask:0xf\n\ts_nop 1\n\t"
            "v_add_f32_dpp v117, v117, v117 row_half_mirror row_mask:0xf bank_mask:0xf\n\t"
            "v_rcp_f32 v121, v117\n\tv_min_u32 v116, -2, v116\n\tv_min_u32 v117, -2, v117\n\tv_mul_f32 v120, v120, v121\n\ts_waitcnt lgkmcnt(0)\n\ts_nop 0\n\t"
            MRG(0, "v100", "v101") MRG(1, "v102", "v103") MRG(2, "v104", "v105") MRG(3, "v106", "v107")
            MRG(4, "v108", "v109") MRG(5, "v110", "v111") MRG(6, "v112", "v113") MRG(7, "v114", "v115")
            A_PACK_QUAD
            : "=&{v[100:103]}"(q), "=&{v[116:117]}"(st) : [rsr] "v"(rsr), [rar] "v"(rar)
            : "v96", "v97", "v104", "v105", "v106", "v107", "v108", "v109", "v110", "v111", "v112", "v113", "v114", "v115", "v118", "v119", "v120", "v121", "memory");
        CST(59);
        st16_sc1(q, oout, rs_ws);
        st8_sc1(st, sout, rs_ws);
      }
      STAMP(6 + it * 12);
    }
    if (role == 3) {
      STAGE_VARS();
      const int hh = 8 * g + w;
      const u32 sl = S_O0 + pbase + (hh * 8 + (c16 & 3)) * O_SLOT;
      u32 o0off = sl + (c16 >> 2) * 64 + g4 * 16, stoff = sl + 256;
      if (it == 0) {
        if (NE3 < 8) {
          lds_barrier();
#pragma unroll
          for (int i = NE3; i < 8; ++i) ROLE_DMA(i);
        }
        STAMP(12);
        WAITVM(0);
        STAMP(15);
      }
      v4u wf[8];
      const u32 wf0 = WF0();
#pragma unroll
      for (int s = 0; s < 4; ++s)
#pragma unroll
        for (int t = 0; t < 2; ++t) wf[s * 2 + t] = wfrag(L + L_WR + w * 8192 + s * 2048 + (wf0 ^ (t * 32)));
      u32 rl = L_RR + red_lane(lane), rw = L_RR + w * 512 + c16 * 64 + g4 * 16;
      u32 xout = (lane & 3) < 2 ? S_X1P0 + pbase + (lane & 3) * 20480 + (g * 20 + r) * 256 + (lane >> 2) * 16 : OOB;
      f2 res = {0.f, 0.f};
      if (g == 0) { const u32 x = *(LAS u32*)(L + L_XR + g4 * 1280 + (32 * r + 2 * c16) * 2); res = f2{bflo(x), bfhi(x)}; }
#pragma unroll
      for (int i = 0; i < 8; ++i) asm volatile("" : "+a"(wf[i]));
      LAUNDER_V(o0off); LAUNDER_V(stoff); LAUNDER_V(rl); LAUNDER_V(rw); LAUNDER_V(xout); LAUNDER_V(res);
      if (it == 1) idle<SLP_O>();
      STAMP1(34);
      {
        u32 cnt = SPIN_MAX;
#define S3_MIX(j, oa, ob, d) \
            "v_lshlrev_b32 v116, 16, " oa "\n\tv_and_b32 v117, 0xffff0000, " oa "\n\tv_lshlrev_b32 v118, 16, " ob "\n\tv_and_b32 v119, 0xffff0000, " ob "\n\t" \
            "v_pk_mul_f32 v[118:119], v[118:119], v[110:111] op_sel_hi:[1,0]\n\tv_pk_fma_f32 v[116:117], v[116:117], v[108:109], v[118:119] op_sel_hi:[1,0,1]\n\t" \
            "v_cvt_pk_bf16_f32 " d ", v116, v117\n\t"
        asm volatile(A_TS(0) A_POLL
            "buffer_load_dwordx4 v[96:99], %[oo], %[rs], 0 offen sc1\n\tbuffer_load_dwordx4 v[100:103], %[oo], %[rs], 0 offen offset:1536 sc1\n\t"
            "buffer_load_dwordx2 v[104:105], %[os], %[rs], 0 offen sc1\n\tbuffer_load_dwordx2 v[106:107], %[os], %[rs], 0 offen offset:1536 sc1\n\ts_waitcnt vmcnt(0)\n\t"
            "v_max3_u32 v112, v96, v97, v98\n\tv_max3_u32 v113, v99, v100, v101\n\tv_max3_u32 v114, v102, v103, v104\n\tv_max3_u32 v115, v105, v106, v107\n\t"
            "v_max3_u32 v112, v112, v113, v114\n\tv_max_u32 v112, v112, v115\n\t"
            A_DONE("v112") A_TS(1)
            "v_max_f32 v112, v104, v106\n\tv_sub_f32 v113, v104, v112\n\tv_sub_f32 v114, v106, v112\n\tv_exp_f32 v113, v113\n\tv_exp_f32 v114, v114\n\ts_nop 0\n\t"
            "v_mul_f32 v113, v113, v105\n\tv_mul_f32 v114, v114, v107\n\tv_add_f32 v112, v113, v114\n\tv_rcp_f32 v112, v112\n\ts_nop 0\n\t"
            "v_mul_f32 v108, v113, v112\n\tv_mul_f32 v110, v114, v112\n\t"
            S3_MIX(0, "v96", "v100", "v112") S3_MIX(1, "v97", "v101", "v113") S3_MIX(2, "v98", "v102", "v114") S3_MIX(3, "v99", "v103", "v115")
            "s_nop 1\n\t"
            MF("v[96:99]", "%[w0]", "v[112:115]", "0") MVT("v112", "v113", "v114", "v115", "4") MF("v[100:103]", "%[w1]", "v[112:115]", "0") MVU("v112", "v113", "v114", "v115", "8")
            MF("v[96:99]", "%[w2]", RT, "v[96:99]") MF("v[100:103]", "%[w3]", RT, "v[100:103]") MVT("v112", "v113", "v114", "v115", "12")
            MF("v[96:99]", "%[w4]", RU, "v[96:99]") MF("v[100:103]", "%[w5]", RU, "v[100:103]") MF("v[96:99]", "%[w6]", RT, "v[96:99]") MF("v[100:103]", "%[w7]", RT, "v[100:103]")
            "s_nop 5\n\t" A_TS(2) A_EXEC_C4 "ds_write_b128 %[rw], v[96:99]\n\tds_write_b128 %[rw], v[100:103] offset:256\n\t" A_EXEC_ALL A_BAR
            : [cnt] "+s"(cnt)
            : [oo] "v"(o0off), [os] "v"(stoff), [rw] "v"(rw), [rs] "s"(rs_ws),
              [w0] "a"(wf[0]), [w1] "a"(wf[1]), [w2] "a"(wf[2]), [w3] "a"(wf[3]), [w4] "a"(wf[4]), [w5] "a"(wf[5]), [w6] "a"(wf[6]), [w7] "a"(wf[7])
            : Z32_CLOBBER, "vcc", "scc", "memory" TS_CLOBBER);
        TS_FLUSH(60);
      }
      STAMP1(29);
      if (w == 0) {
        v4u q;
        asm volatile(A_SUM8 A_PACK_QUAD : "=&{v[100:103]}"(q) : [rl] "v"(rl), [res] "v"(res) : Z28_CLOBBER, "memory");
        st16_sc1(q, xout, rs_ws);
      }
    } else if (role == 4) {
      STAGE_VARS();
      const int j4 = (g - 4) * 27 + r;
      const u32 wf0 = WF0() ^ ((w & 1) * 32);
      const u32 wbase = L_WR + ((w >> 1) & 1) * 40960 + (w >> 2) * 20480;
      v4u wf[10];
      if (it == 0) {
        lds_barrier();
#pragma unroll
        for (int i = NE; i < 10; ++i) ROLE_DMA(i);
        WAITVM(0);
        lds_barrier();
#pragma unroll
        for (int s = 0; s < 10; ++s) wf[s] = wfrag(L + wbase + s * 2048 + wf0);
#pragma unroll
        for (int i = 0; i < 10; ++i) asm volatile("" : "+a"(wf[i]));
        const v4u one = {ONES, ONES, ONES, ONES};
        f4 c0;
        asm volatile("s_nop 1\n\t" MF("%0", "%1", "%11", "0") MF("%0", "%2", "%11", "%0") MF("%0", "%3", "%11", "%0") MF("%0", "%4", "%11", "%0") MF("%0", "%5", "%11", "%0")
                     MF("%0", "%6", "%11", "%0") MF("%0", "%7", "%11", "%0") MF("%0", "%8", "%11", "%0") MF("%0", "%9", "%11", "%0") MF("%0", "%10", "%11", "%0") "s_nop 7"
                     : "=&v"(c0)
                     : "a"(wf[0]), "a"(wf[1]), "a"(wf[2]), "a"(wf[3]), "a"(wf[4]), "a"(wf[5]), "a"(wf[6]), "a"(wf[7]), "a"(wf[8]), "a"(wf[9]), "v"(one));
        if (c16 == 0) *(LAS f4*)(L + L_CP + (w >> 2) * 256 + (w & 3) * 64 + g4 * 16) = c0;
        lds_barrier();
        if (w == 1 && lane < 32) *(LAS f2*)(L + L_CS + 128 + lane * 8) = *(LAS f2*)(L + L_CP + lane * 8) + *(LAS f2*)(L + L_CP + 256 + lane * 8);
      } else {
        idle<SLP_S4>();
#pragma unroll
        for (int s = 0; s < 10; ++s) wf[s] = wfrag(L + wbase + s * 2048 + wf0);
#pragma unroll
        for (int i = 0; i < 10; ++i) asm volatile("" : "+a"(wf[i]));
      }
      u32 rl = L_RR + red_lane(lane), hout = (lane & 3) ? OOB : S_HB0 + pbase + j4 * 256 + lane * 4;
      u32 xaddr = L_XN + (c16 & 3) * XN_STRIDE + ((w >> 2) * 320 + 8 * g4) * 2;
      u32 rw = L_RR + (w >> 2) * 1024 + ((w >> 1) & 1) * 512 + (w & 1) * 256 + c16 * 64 + g4 * 16;
      u32 ra = L_XN + (w & 3) * XN_STRIDE + lane * 16, rb = lane < 16 ? L_XN + (w & 3) * XN_STRIDE + 1024 + lane * 16 : L_ZERO;
      u32 stw = L_ST + (w & 3) * 8, str = L_ST + g4 * 8, csr = L_CS + 128 + c16 * 8;
      LAUNDER_V(rl); LAUNDER_V(hout); LAUNDER_V(xaddr); LAUNDER_V(rw); LAUNDER_V(ra); LAUNDER_V(rb); LAUNDER_V(stw); LAUNDER_V(str); LAUNDER_V(csr);
      if (w < 5) {
        const u32 u = w * 64 + lane;
        u32 o1 = S_X1P0 + pbase + (g - 4) * 20480 + u * 16;
        u32 xw = L_XN + ((u >> 2) & 3) * XN_STRIDE + (u >> 4) * 64 + (u & 3) * 16;
        LAUNDER_V(o1); LAUNDER_V(xw);
        u32 cnt = SPIN_MAX;
        STAMP1(35);
#define S4_D0(d, p, sel) "v_dot2_f32_bf16 " d ", " p ", " sel ", 0\n\t"
#define S4_DC(d, p, sel) "v_dot2c_f32_bf16 " d ", " sel ", " p "\n\t"
#define S4_ROW0(a, b, c, d) S4_D0("v112", a, "%[lo]") S4_D0("v113", a, "%[hi]") S4_D0("v114", b, "%[lo]") S4_D0("v115", b, "%[hi]") \
                            S4_D0("v116", c, "%[lo]") S4_D0("v117", c, "%[hi]") S4_D0("v118", d, "%[lo]") S4_D0("v119", d, "%[hi]")
#define S4_ROW(a, b, c, d) S4_DC("v112", a, "%[lo]") S4_DC("v113", a, "%[hi]") S4_DC("v114", b, "%[lo]") S4_DC("v115", b, "%[hi]") \
                           S4_DC("v116", c, "%[lo]") S4_DC("v117", c, "%[hi]") S4_DC("v118", d, "%[lo]") S4_DC("v119", d, "%[hi]")
        asm volatile(A_POLL
            "buffer_load_dwordx4 v[96:99], %[o], %[rs], 0 offen sc1\n\tbuffer_load_dwordx4 v[100:103], %[o], %[rs], %[q1] offen sc1\n\t"
            "buffer_load_dwordx4 v[104:107], %[o], %[rs], %[q2] offen sc1\n\tbuffer_load_dwordx4 v[108:111], %[o], %[rs], %[q3] offen sc1\n\ts_waitcnt vmcnt(0)\n\t"
            "v_max3_u32 v112, v96, v97, v98\n\tv_max3_u32 v113, v99, v100, v101\n\tv_max3_u32 v114, v102, v103, v104\n\tv_max3_u32 v115, v105, v106, v107\n\t"
            "v_max3_u32 v116, v108, v109, v110\n\tv_max3_u32 v112, v112, v113, v111\n\tv_max3_u32 v114, v114, v115, v116\n\tv_max_u32 v112, v112, v114\n\t"
            A_DONE("v112")
            S4_ROW0("v96", "v97", "v98", "v99") S4_ROW("v100", "v101", "v102", "v103") S4_ROW("v104", "v105", "v106", "v107") S4_ROW("v108", "v109", "v110", "v111")
            "v_cvt_pk_bf16_f32 v96, v112, v113\n\tv_cvt_pk_bf16_f32 v97, v114, v115\n\tv_cvt_pk_bf16_f32 v98, v116, v117\n\tv_cvt_pk_bf16_f32 v99, v118, v119\n\t"
            "ds_write_b128 %[xw], v[96:99]"
            : [cnt] "+s"(cnt)
            : [o] "v"(o1), [xw] "v"(xw), [rs] "s"(rs_ws), [q1] "s"(5120u), [q2] "s"(10240u), [q3] "s"(15360u), [lo] "s"(0x00003F80u), [hi] "s"(0x3F800000u)
            : Z32_CLOBBER, "vcc", "scc", "memory");
        STAMP(8 + it * 12);
      }
#define XRD(d, off) "ds_read_b128 " d ", %[xa] offset:" off "\n\t"
#define LGK(n) "s_waitcnt lgkmcnt(" n ")\n\t"
#define S4_MF(wi, b) "v_mfma_f32_16x16x32_bf16 %[acc], %[" wi "], " b ", %[acc]\n\t"
#define S4_MF0(wi, b) "v_mfma_f32_16x16x32_bf16 %[acc], %[" wi "], " b ", 0\n\t"
#define S4_HEAD XRD("v[104:107]", "0") XRD("v[108:111]", "64") XRD("v[112:115]", "128") XRD("v[116:119]", "192") XRD("v[120:123]", "256") XRD("v[124:127]", "320")
#define S4_W [w0] "a"(wf[0]), [w1] "a"(wf[1]), [w2] "a"(wf[2]), [w3] "a"(wf[3]), [w4] "a"(wf[4]), [w5] "a"(wf[5]), [w6] "a"(wf[6]), [w7] "a"(wf[7]), \
             [w8] "a"(wf[8]), [w9] "a"(wf[9]), [xa] "v"(xaddr), [rw] "v"(rw)
      f4 acc;
      if (w < 4) {
        float s1, s2, t1, t2;
        asm volatile(A_BAR "\n\t"
            "ds_read_b128 v[96:99], %[ra]\n\tds_read_b128 v[100:103], %[rb]\n\t" S4_HEAD
            "v_mov_b32 %[s1], 0\n\tv_mov_b32 %[s2], 0\n\t"
            LGK("6") DS1("v96") DS1("v97") LGK("5") S4_MF0("w0", "v[104:107]") XRD("v[104:107]", "384")
            DS1("v98") DS1("v99") LGK("5") S4_MF("w1", "v[108:111]") XRD("v[108:111]", "448")
            DS1("v100") DS1("v101") LGK("5") S4_MF("w2", "v[112:115]") XRD("v[112:115]", "512")
            DS1("v102") DS1("v103") LGK("5") S4_MF("w3", "v[116:119]") XRD("v[116:119]", "576")
            "s_nop 1\n\t" RED2("row_ror:8") LGK("5") S4_MF("w4", "v[120:123]")
            RED2("row_ror:4") LGK("4") S4_MF("w5", "v[124:127]")
            RED2("row_ror:2") LGK("3") S4_MF("w6", "v[104:107]")
            RED2("row_ror:1") LGK("2") S4_MF("w7", "v[108:111]")
            SWP2("v_permlane16_swap_b32") LGK("1") S4_MF("w8", "v[112:115]")
            SWP2("v_permlane32_swap_b32") LGK("0") S4_MF("w9", "v[116:119]")
            "v_mul_f32 v96, 0x3acccccd, %[s1]\n\tv_mul_f32 v97, 0x3acccccd, %[s2]\n\tv_fma_f32 v97, -v96, v96, v97\n\tv_max_f32 v97, 0, v97\n\t"
            "v_add_f32 v97, 0x3727c5ac, v97\n\tv_rsq_f32 v97, v97\n\t"
            "s_mov_b64 exec, 1\n\tds_write_b64 %[stw], v[96:97]\n\t"
            "s_nop 0\n\t" A_EXEC_C4 "ds_write_b128 %[rw], %[acc]\n\t" A_EXEC_ALL A_BAR
            : [acc] "=&v"(acc), [s1] "=&v"(s1), [s2] "=&v"(s2), [t1] "=&v"(t1), [t2] "=&v"(t2)
            : S4_W, [ra] "v"(ra), [rb] "v"(rb), [stw] "v"(stw), [one] "s"(ONES)
            : Z32_CLOBBER, "memory");
      } else {
        asm volatile(A_BAR "\n\t" S4_HEAD
            LGK("5") S4_MF0("w0", "v[104:107]") XRD("v[104:107]", "384")
            LGK("5") S4_MF("w1", "v[108:111]") XRD("v[108:111]", "448")
            LGK("5") S4_MF("w2", "v[112:115]") XRD("v[112:115]", "512")
            LGK("5") S4_MF("w3", "v[116:119]") XRD("v[116:119]", "576")
            LGK("5") S4_MF("w4", "v[120:123]") LGK("4") S4_MF("w5", "v[124:127]") LGK("3") S4_MF("w6", "v[104:107]")
            LGK("2") S4_MF("w7", "v[108:111]") LGK("1") S4_MF("w8", "v[112:115]") LGK("0") S4_MF("w9", "v[116:119]")
            "s_nop 5\n\t" A_EXEC_C4 "ds_write_b128 %[rw], %[acc]\n\t" A_EXEC_ALL A_BAR
            : [acc] "=&v"(acc) : S4_W : Z32_CLOBBER, "memory");
      }
      STAMPB(27);
      if (w == 0) {
        v4u q;
        asm volatile(
            "ds_read2st64_b64 v[96:99], %[rl] offset0:0 offset1:2\n\tds_read2st64_b64 v[104:107], %[rl] offset0:1 offset1:3\n\t"
            "ds_read_b64 v[108:109], %[str]\n\tds_read2_b64 v[112:115], %[csr] offset0:0 offset1:16\n\t"
            "s_waitcnt lgkmcnt(3)\n\t" A_PKADD("v[96:97]", "v[96:97]", "v[98:99]")
            "s_waitcnt lgkmcnt(2)\n\t" A_PKADD("v[104:105]", "v[104:105]", "v[106:107]")
            "s_waitcnt lgkmcnt(0)\n\t"
            "v_pk_fma_f32 v[96:97], v[112:113], v[108:109], v[96:97] op_sel_hi:[1,0,1] neg_lo:[0,1,0] neg_hi:[0,1,0]\n\t"
            "v_pk_fma_f32 v[104:105], v[114:115], v[108:109], v[104:105] op_sel_hi:[1,0,1] neg_lo:[0,1,0] neg_hi:[0,1,0]\n\t"
            "v_pk_mul_f32 v[96:97], v[96:97], v[108:109] op_sel:[0,1] op_sel_hi:[1,1]\n\t"
            "v_pk_mul_f32 v[104:105], v[104:105], v[108:109] op_sel:[0,1] op_sel_hi:[1,1]\n\t"
            "v_cmp_lt_f32 vcc, 0, v96\n\tv_mul_f32 v98, 0, v96\n\tv_mul_f32 v99, 0, v97\n\tv_cndmask_b32 v96, v98, v96, vcc\n\t"
            "v_cmp_lt_f32 vcc, 0, v97\n\ts_nop 1\n\tv_cndmask_b32 v97, v99, v97, vcc\n\t"
            "v_pk_mul_f32 v[96:97], v[96:97], v[104:105]\n\t"
            A_PACK_QUAD
            : "=&{v[100:103]}"(q) : [rl] "v"(rl), [str] "v"(str), [csr] "v"(csr) : Z28_CLOBBER, "vcc", "memory");
        st16_sc1(q, hout, rs_ws);
      }
    } else if (role == 5) {
      STAGE_VARS();
      const int s0 = w < 6 ? 7 * w : 42 + 6 * (w - 6);
      if (it == 0) {
        lds_barrier();
#pragma unroll
        for (int i = NE; i < 12; ++i) ROLE_DMA(i);
        if (w < 6) { ROLE_DMA(12); ROLE_DMA(13); }
        WAITVM(0);
      }
      idle<SLP_S5>();
      v4u wf[14];
      const u32 wf0 = WF0();
#pragma unroll
      for (int s = 0; s < 6; ++s)
#pragma unroll
        for (int t = 0; t < 2; ++t) wf[s * 2 + t] = wfrag(L + L_WR + (s0 + s) * 2048 + (wf0 ^ (t * 32)));
      {
        const int s6 = w < 6 ? s0 + 6 : s0;
        wf[12] = wfrag(L + L_WR + s6 * 2048 + wf0); wf[13] = wfrag(L + L_WR + s6 * 2048 + (wf0 ^ 32));
      }
#pragma unroll
      for (int i = 0; i < 14; ++i) asm volatile("" : "+a"(wf[i]));
      const int slot = c16 >> 2;
      const u32 hoff = S_HB0 + pbase + (c16 & 3) * 64 + g4 * 16;
      u32 oA = hoff + (s0 + slot) * 256;
      u32 oB = (slot < (w < 6 ? 3 : 2)) ? hoff + (s0 + 4 + slot) * 256 : OOB;
      u32 rl = L_RR + red_lane(lane), rw = L_RR + w * 512 + c16 * 64 + g4 * 16;
      u32 xout = it == 0 ? S_X2 + (lane & 3) * 5120 + r * 256 + (lane >> 2) * 16 : ((lane & 3) ? OOB : (u32)(g4 * 1280 + r * 64 + (c16 >> 2) * 16));
      f2 res = {0.f, 0.f};
      if (w == 0) {
        const u32 rxo = S_X1P0 + pbase + r * 256 + lane * 4;
        u32 rx0, rx1, rx2, rx3;
        _Pragma("nounroll") for (u32 spin = 0; spin < SPIN_MAX; ++spin) {
          asm volatile("buffer_load_dword %0, %4, %5, 0 offen sc1\n\tbuffer_load_dword %1, %4, %5, %6 offen sc1\n\tbuffer_load_dword %2, %4, %5, %7 offen sc1\n\tbuffer_load_dword %3, %4, %5, %8 offen sc1\n\ts_waitcnt vmcnt(0)"
                       : "=&v"(rx0), "=&v"(rx1), "=&v"(rx2), "=&v"(rx3) : "v"(rxo), "s"(rs_ws), "s"(5120u), "s"(10240u), "s"(15360u) : "memory");
          if (all_valid(umax3(umax3(rx0, rx1, rx2), rx3, 0u))) break;
          __builtin_amdgcn_s_sleep(2);
        }
        res = f2{(bflo(rx0) + bflo(rx1)) + (bflo(rx2) + bflo(rx3)), (bfhi(rx0) + bfhi(rx1)) + (bfhi(rx2) + bfhi(rx3))};
      }
      LAUNDER_V(oA); LAUNDER_V(oB); LAUNDER_V(rl); LAUNDER_V(rw); LAUNDER_V(xout); LAUNDER_V(res);
      STAMP1(36);
      {
        u32 cnt = SPIN_MAX;
        asm volatile(A_POLL
            "buffer_load_dwordx4 v[96:99], %[oa], %[rs], 0 offen sc1\n\tbuffer_load_dwordx4 v[100:103], %[ob], %[rs], 0 offen sc1\n\ts_waitcnt vmcnt(0)\n\t"
            "v_max3_u32 v112, v96, v97, v98\n\tv_max3_u32 v113, v99, v100, v101\n\tv_max3_u32 v112, v112, v102, v103\n\tv_max_u32 v112, v112, v113\n\t"
            A_DONE("v112")
            MF("v[104:107]", "%[w0]", "v[96:99]", "0") MVT("v96", "v97", "v98", "v99", "4") MF("v[108:111]", "%[w1]", "v[96:99]", "0") MVU("v96", "v97", "v98", "v99", "8")
            MF("v[104:107]", "%[w2]", RT, "v[104:107]") MF("v[108:111]", "%[w3]", RT, "v[108:111]") MVT("v96", "v97", "v98", "v99", "12")
            MF("v[104:107]", "%[w4]", RU, "v[104:107]") MF("v[108:111]", "%[w5]", RU, "v[108:111]") MVU("v100", "v101", "v102", "v103", "4")
            MF("v[104:107]", "%[w6]", RT, "v[104:107]") MF("v[108:111]", "%[w7]", RT, "v[108:111]") MVT("v100", "v101", "v102", "v103", "8")
            MF("v[104:107]", "%[w8]", "v[100:103]", "v[104:107]") MF("v[108:111]", "%[w9]", "v[100:103]", "v[108:111]")
            MF("v[104:107]", "%[w10]", RU, "v[104:107]") MF("v[108:111]", "%[w11]", RU, "v[108:111]")
            MF("v[104:107]", "%[w12]", RT, "v[104:107]") MF("v[108:111]", "%[w13]", RT, "v[108:111]")
            "s_nop 5\n\t" A_EXEC_C4 "ds_write_b128 %[rw], v[104:107]\n\tds_write_b128 %[rw], v[108:111] offset:256\n\t" A_EXEC_ALL A_BAR
            : [cnt] "+s"(cnt)
            : [oa] "v"(oA), [ob] "v"(oB), [rw] "v"(rw), [rs] "s"(rs_ws),
              [w0] "a"(wf[0]), [w1] "a"(wf[1]), [w2] "a"(wf[2]), [w3] "a"(wf[3]), [w4] "a"(wf[4]), [w5] "a"(wf[5]), [w6] "a"(wf[6]),
              [w7] "a"(wf[7]), [w8] "a"(wf[8]), [w9] "a"(wf[9]), [w10] "a"(wf[10]), [w11] "a"(wf[11]), [w12] "a"(wf[12]), [w13] "a"(wf[13])
            : Z32_CLOBBER, "vcc", "scc", "memory");
      }
      STAMP1(31);
      if (w == 0) {
        v4u q;
        asm volatile(A_SUM8 A_PACK_QUAD : "=&{v[100:103]}"(q) : [rl] "v"(rl), [res] "v"(res) : Z28_CLOBBER, "memory");
        if (it == 0) {
          st16_sc1(q, xout, rs_ws);
          st16_sc1(q, xout + 4 * 5120, rs_ws);
        } else {
          const rsrc_t rs_o = mk_rsrc(outp, 4 * DM * 2);
          asm volatile("s_nop 4\n\tbuffer_store_dwordx4 %0, %1, %2, 0 offen\n\ts_nop 1" ::"v"(q), "v"(xout), "s"(rs_o) : "memory");
          if (r == 0) st8_sc1(v2u{tag_lo, tag_hi}, lane == 0 ? WS_G : OOB, rs_ws);
        }
      }
    }
    STAMP(10 + it * 12);
  }
  STAMP_OUT();
}

}

extern "C" void kernel_launch(void* const* d_in, const int* in_sizes, int n_in, void* d_out, int out_size, void* d_ws,
                              size_t ws_size, hipStream_t stream) {
  Args a;
  a.x = (const u16*)d_in[0];
  a.k = (const u16*)d_in[1];
  a.v = (const u16*)d_in[2];
  a.wq = (const u16*)d_in[3];
  a.wo = (const u16*)d_in[4];
  a.wg = (const u16*)d_in[6];
  a.wu = (const u16*)d_in[7];
  a.wd = (const u16*)d_in[8];
  a.out = (u16*)d_out;
  a.ws = (unsigned char*)d_ws;
  (void)hipFuncSetAttribute((const void*)fused_decode, hipFuncAttributeMaxDynamicSharedMemorySize, (int)L_END);
  hipLaunchKernelGGL(fused_decode, dim3(NWG), dim3(NT), L_END, stream, a);
}
```

```cpp
#include <hip/hip_runtime.h>
#include <hip/hip_bf16.h>

typedef unsigned int u32;
typedef unsigned long long u64;
typedef unsigned short u16;
typedef u32 v4u __attribute__((ext_vector_type(4)));
typedef u32 v2u __attribute__((ext_vector_type(2)));
typedef float f4 __attribute__((ext_vector_type(4)));
typedef float f2 __attribute__((ext_vector_type(2)));
typedef __bf16 bf16x8 __attribute__((ext_vector_type(8)));
typedef short s16x4 __attribute__((ext_vector_type(4)));
#define LAS __attribute__((address_space(3)))

namespace {

constexpr int NWG = 256, NT = 512;
constexpr int DM = 640, SK = 4096, WIN = 1024, DH = 128, FF = 1728;

constexpr u32 WS_G = 0;
constexpr u32 WS_FLAG = 4096;
constexpr u32 WS_Q1 = 8192;
constexpr u32 WS_S = 524288;
constexpr u32 O_SLOT = 384;
constexpr u32 S_O0 = WS_S, S_X1P0 = S_O0 + 256 * O_SLOT, S_HB0 = S_X1P0 + 81920, S_X2 = S_HB0 + 2 * 13824;
constexpr u32 S_O1 = S_X2 + 40960;
constexpr u32 PASS_STRIDE = S_O1 - S_O0;
constexpr u32 S_Q2 = S_O1 + (S_X2 - S_O0);
constexpr u32 WS_END = WS_S + 256 * 2560;
static_assert(S_Q2 + 131072 <= WS_END, "sentinel region too small");
static_assert((S_X1P0 % 128) == 0 && (S_X2 % 128) == 0 && (S_O1 % 128) == 0 && (S_Q2 % 128) == 0 && (S_HB0 % 128) == 0, "alignment");
constexpr u32 WS_BYTES = 4u << 20;
constexpr u32 OOB = 0x80000000u;

constexpr u32 L_WR = 0;
constexpr u32 L_WQ = 110592;
constexpr u32 L_XN = 131072;
constexpr u32 XN_STRIDE = 1344;
constexpr u32 L_ONE = L_XN + 4 * XN_STRIDE + 32;
constexpr u32 L_XS = L_ONE + XN_STRIDE;
constexpr u32 L_RA = L_XS + 20 * 320;
constexpr u32 L_RR = L_RA + 4096 + 128;
constexpr u32 L_RQ = L_RR + 8192;
constexpr u32 L_CS = L_RQ + 1024;
constexpr u32 L_ST = L_CS + 256;
constexpr u32 L_GR = L_ST + 128;
constexpr u32 L_QB = L_GR + 512;
constexpr u32 L_ZERO = L_QB + 2048;
constexpr u32 L_END = L_ZERO + 16;
static_assert(L_END <= 160 * 1024 && L_END > 80 * 1024, "LDS");

constexpr u32 SENT = 0xFFFFFFFFu;
constexpr u32 SPIN_MAX = 1u << 16;

#define RX 0
#define POLL_SLEEP 0
#define SLP_O 0
#define SLP_Q 0
#define SLP_S5_0 10
#define SLP_S4 20
#define SLP_S5 50
#define SLP_X_NONE 90
#define SLP_X_S3 55
#define SLP_X_S4 20

struct Args {
  const u16* x; const u16* k; const u16* v; const u16* wq; const u16* wo;
  const u16* wg; const u16* wu; const u16* wd; u16* out; unsigned char* ws;
};

typedef __amdgpu_buffer_rsrc_t rsrc_t;
__device__ __forceinline__ rsrc_t mk_rsrc(const void* p, u32 bytes) { return __builtin_amdgcn_make_buffer_rsrc((void*)p, 0, (int)bytes, 0x00020000); }

__device__ __forceinline__ float bflo(u32 d) { return __builtin_bit_cast(float, d << 16); }
__device__ __forceinline__ float bfhi(u32 d) { return __builtin_bit_cast(float, d & 0xffff0000u); }
__device__ __forceinline__ u32 cvtpk(float lo, float hi) {
  typedef __bf16 bf16x2 __attribute__((ext_vector_type(2)));
  typedef float f32x2 __attribute__((ext_vector_type(2)));
  f32x2 v = {lo, hi};
  return __builtin_bit_cast(u32, __builtin_convertvector(v, bf16x2));
}
__device__ __forceinline__ u32 nosent(u32 d) { return d < 0xFFFFFFFEu ? d : 0xFFFFFFFEu; }
template <int CTRL> __device__ __forceinline__ float dpp(float x) {
  return __builtin_bit_cast(float, __builtin_amdgcn_mov_dpp(__builtin_bit_cast(int, x), CTRL, 0xf, 0xf, true));
}
template <int CTRL> __device__ __forceinline__ u32 dppu(u32 x) { return (u32)__builtin_amdgcn_mov_dpp((int)x, CTRL, 0xf, 0xf, true); }
template <int RM, int BM> __device__ __forceinline__ float sel(float old, float src) {
  return __builtin_bit_cast(float, __builtin_amdgcn_update_dpp(__builtin_bit_cast(int, old), __builtin_bit_cast(int, src), 0xE4, RM, BM, false));
}
constexpr int ROR8 = 0x128, ROR4 = 0x124, ROR2 = 0x122, ROR1 = 0x121, SHL1 = 0x101, SHL4 = 0x104, SHL8 = 0x108, SHL12 = 0x10C;
__device__ __forceinline__ float wave_sum(float x) {
  x += dpp<ROR8>(x); x += dpp<ROR4>(x); x += dpp<ROR2>(x); x += dpp<ROR1>(x);
  auto s = __builtin_amdgcn_permlane16_swap(__float_as_uint(x), __float_as_uint(x), false, false);
  x = __uint_as_float(s[0]) + __uint_as_float(s[1]);
  auto t = __builtin_amdgcn_permlane32_swap(__float_as_uint(x), __float_as_uint(x), false, false);
  return __uint_as_float(t[0]) + __uint_as_float(t[1]);
}
__device__ __forceinline__ float wave_max(float x) {
  x = fmaxf(x, dpp<ROR8>(x)); x = fmaxf(x, dpp<ROR4>(x)); x = fmaxf(x, dpp<ROR2>(x)); x = fmaxf(x, dpp<ROR1>(x));
  auto s = __builtin_amdgcn_permlane16_swap(__float_as_uint(x), __float_as_uint(x), false, false);
  x = fmaxf(__uint_as_float(s[0]), __uint_as_float(s[1]));
  auto t = __builtin_amdgcn_permlane32_swap(__float_as_uint(x), __float_as_uint(x), false, false);
  return fmaxf(__uint_as_float(t[0]), __uint_as_float(t[1]));
}
__device__ __forceinline__ f4 mfma(v4u a, v4u b, f4 c) {
  return __builtin_amdgcn_mfma_f32_16x16x32_bf16(__builtin_bit_cast(bf16x8, a), __builtin_bit_cast(bf16x8, b), c, 0, 0, 0);
}
__device__ __forceinline__ u32 umax3(u32 a, u32 b, u32 c) { u32 m = a > b ? a : b; return m > c ? m : c; }
__device__ __forceinline__ u32 umax4(v4u v) { return umax3(v.x, v.y, v.z > v.w ? v.z : v.w); }
__device__ __forceinline__ bool all_valid(u32 mx) { return __builtin_amdgcn_ballot_w64(mx == SENT) == 0ull; }
__device__ __forceinline__ void lds_barrier() {
  asm volatile("s_waitcnt lgkmcnt(0)" ::: "memory");
  __builtin_amdgcn_s_barrier();
  asm volatile("" ::: "memory");
}
template <int PITCH = 64> __device__ __forceinline__ v4u wfrag(LAS unsigned char* p) {
  typedef short v4i16_t __attribute__((ext_vector_type(4)));
  v4i16_t lo = __builtin_amdgcn_ds_read_tr16_b64_v4i16((LAS v4i16_t*)p);
  v4i16_t hi = __builtin_amdgcn_ds_read_tr16_b64_v4i16((LAS v4i16_t*)(p + 4 * PITCH));
  v2u a = __builtin_bit_cast(v2u, lo), b = __builtin_bit_cast(v2u, hi);
  return v4u{a.x, a.y, b.x, b.y};
}
__device__ __forceinline__ v4u wfrag2(LAS unsigned char* p, LAS unsigned char* q) {
  typedef short v4i16_t __attribute__((ext_vector_type(4)));
  v4i16_t lo = __builtin_amdgcn_ds_read_tr16_b64_v4i16((LAS v4i16_t*)p);
  v4i16_t hi = __builtin_amdgcn_ds_read_tr16_b64_v4i16((LAS v4i16_t*)q);
  v2u a = __builtin_bit_cast(v2u, lo), b = __builtin_bit_cast(v2u, hi);
  return v4u{a.x, a.y, b.x, b.y};
}
#define WAITVM(n) asm volatile("s_waitcnt vmcnt(" #n ")" ::: "memory")
#define WAITVM_(n) WAITVM(n)
#define WAITVM_I(n) asm volatile("s_waitcnt vmcnt(%0)" ::"n"(n) : "memory")
__device__ __forceinline__ void dma16(u32 ldsaddr, u32 voff, rsrc_t rs, u32 soff) {
  asm volatile("s_mov_b32 m0, %0\n\ts_nop 0\n\tbuffer_load_dwordx4 %1, %2, %3 offen nt lds" ::"s"(ldsaddr), "v"(voff), "s"(rs), "s"(soff) : "memory");
}
__device__ __forceinline__ void st16_sc1(v4u d, u32 voff, rsrc_t rs) {
  asm volatile("buffer_store_dwordx4 %0, %1, %2, 0 offen sc1\n\ts_nop 1" ::"v"(d), "v"(voff), "s"(rs) : "memory");
}
__device__ __forceinline__ void st8_sc1(v2u d, u32 voff, rsrc_t rs) {
  asm volatile("buffer_store_dwordx2 %0, %1, %2, 0 offen sc1\n\ts_nop 1" ::"v"(d), "v"(voff), "s"(rs) : "memory");
}
__device__ __forceinline__ v4u quad_gather(u32 d) {
  return v4u{dppu<0x00>(d), dppu<0x55>(d), dppu<0xAA>(d), dppu<0xFF>(d)};
}
template <int N> __device__ __forceinline__ void idle() {
  if (N > 0) __builtin_amdgcn_s_sleep(N > 127 ? 127 : N);
  if constexpr (N > 127) idle<N - 127>();
}

#define LAUNDER_V(x) asm volatile("" : "+v"(x))
#define LAUNDER_S(x) asm volatile("" : "+s"(x))
typedef const __attribute__((address_space(4))) Args* kargs_t;
#define STAGE_VARS() int lane = lane_; LAUNDER_V(lane); const int g4 = lane >> 4, c16 = lane & 15; (void)g4; (void)c16; \
    int w = w_, c = c_; LAUNDER_S(w); LAUNDER_S(c); const int g = c & 7, r = c >> 3; (void)g; (void)r; \
    const int h = 4 * g + ((r ^ RX) >> 3), bh = (r ^ RX) & 7, b = bh >> 1, half = bh & 1; (void)h; (void)b; (void)half; \
    kargs_t ka = ka_; LAUNDER_S(ka); (void)ka

constexpr u32 ONES = 0x3F803F80u;
__device__ __forceinline__ float dot2(u32 a, u32 b, float c) {
  typedef __bf16 bf16x2 __attribute__((ext_vector_type(2)));
  return __builtin_amdgcn_fdot2_f32_bf16(__builtin_bit_cast(bf16x2, a), __builtin_bit_cast(bf16x2, b), c, false);
}
__device__ __forceinline__ v4u sum_selector(int c16) {
  const u32 one = c16 < 8 ? (0x3F80u << (16 * (c16 & 1))) : 0u;
  const int d = c16 >> 1;
  return v4u{d == 0 ? one : 0u, d == 1 ? one : 0u, d == 2 ? one : 0u, d == 3 ? one : 0u};
}
__device__ __forceinline__ f2 row_stats(v4u xa, v4u xb) {
  float s1 = 0.f, s2 = 0.f, t1 = 0.f, t2 = 0.f;
#pragma unroll
  for (int j = 0; j < 4; ++j) { s1 = dot2(xa[j], ONES, s1); s2 = dot2(xa[j], xa[j], s2); t1 = dot2(xb[j], ONES, t1); t2 = dot2(xb[j], xb[j], t2); }
  const float mean = wave_sum(s1 + t1) * (1.f / DM);
  const float var = fmaxf(fmaf(-mean, mean, wave_sum(s2 + t2) * (1.f / DM)), 0.f);
  return f2{mean, __builtin_amdgcn_rsqf(var + 1e-5f)};
}
__device__ __forceinline__ u32 red_lane(int lane) { return ((lane >> 3) & 1) * 256 + (lane >> 4) * 64 + (lane & 7) * 8; }
template <int NP, int STRIDE> __device__ __forceinline__ f2 red_sum(LAS unsigned char* p) {
  f2 v[NP];
#pragma unroll
  for (int i = 0; i < NP; ++i) v[i] = *(LAS f2*)(p + i * STRIDE);
#pragma unroll
  for (int st = 1; st < NP; st *= 2)
#pragma unroll
    for (int i = 0; i + st < NP; i += 2 * st) v[i] += v[i + st];
  return v[0];
}
#define MF(d, a, b, c) "v_mfma_f32_16x16x32_bf16 " d ", " a ", " b ", " c "\n\t"
#define MV1(d, s, n) "v_mov_b32_dpp " d ", " s " row_shl:" n " row_mask:0xf bank_mask:0xf bound_ctrl:1\n\t"
#define MVT(s0, s1, s2, s3, n) MV1("v120", s0, n) MV1("v121", s1, n) MV1("v122", s2, n) MV1("v123", s3, n)
#define MVU(s0, s1, s2, s3, n) MV1("v124", s0, n) MV1("v125", s1, n) MV1("v126", s2, n) MV1("v127", s3, n)
#define A_POLL ".Lp%=:\n\t"
#define APS 0
#define STR2(x) #x
#define STR(x) STR2(x)
#define A_PAUSE ""
#define A_DONE(t) "v_cmp_eq_u32 vcc, -1, " t "\n\ts_cbranch_vccz .Ld%=\n\t" A_PAUSE "s_sub_u32 %[cnt], %[cnt], 1\n\ts_cbranch_scc0 .Lp%=\n.Ld%=:\n\t"
#define A_EXEC_C4 "s_mov_b32 exec_lo, 0x000f000f\n\ts_mov_b32 exec_hi, 0x000f000f\n\t"
#define A_EXEC_C5 "s_mov_b32 exec_lo, 0x001f001f\n\ts_mov_b32 exec_hi, 0x001f001f\n\t"
#define A_EXEC_ALL "s_mov_b64 exec, -1\n\t"
#define A_BAR "s_waitcnt lgkmcnt(0)\n\ts_barrier"
#define A_PKADD(d, a, b) "v_pk_add_f32 " d ", " a ", " b "\n\t"
#define A_PACK_QUAD "v_cvt_pk_bf16_f32 v96, v96, v97\n\tv_min_u32 v96, -2, v96\n\ts_nop 1\n\t" \
      "v_mov_b32_dpp v100, v96 quad_perm:[0,0,0,0] row_mask:0xf bank_mask:0xf\n\tv_mov_b32_dpp v101, v96 quad_perm:[1,1,1,1] row_mask:0xf bank_mask:0xf\n\t" \
      "v_mov_b32_dpp v102, v96 quad_perm:[2,2,2,2] row_mask:0xf bank_mask:0xf\n\tv_mov_b32_dpp v103, v96 quad_perm:[3,3,3,3] row_mask:0xf bank_mask:0xf"
#define A_SUM8 "ds_read2st64_b64 v[96:99], %[rl] offset0:0 offset1:1\n\tds_read2st64_b64 v[104:107], %[rl] offset0:2 offset1:3\n\t" \
      "ds_read2st64_b64 v[108:111], %[rl] offset0:4 offset1:5\n\tds_read2st64_b64 v[112:115], %[rl] offset0:6 offset1:7\n\t" \
      "s_waitcnt lgkmcnt(3)\n\t" A_PKADD("v[96:97]", "v[96:97]", "v[98:99]") A_PKADD("v[96:97]", "v[96:97]", "%[res]") \
      "s_waitcnt lgkmcnt(2)\n\t" A_PKADD("v[104:105]", "v[104:105]", "v[106:107]") \
      "s_waitcnt lgkmcnt(1)\n\t" A_PKADD("v[108:109]", "v[108:109]", "v[110:111]") A_PKADD("v[96:97]", "v[96:97]", "v[104:105]") \
      "s_waitcnt lgkmcnt(0)\n\t" A_PKADD("v[112:113]", "v[112:113]", "v[114:115]") A_PKADD("v[96:97]", "v[96:97]", "v[108:109]") \
      A_PKADD("v[96:97]", "v[96:97]", "v[112:113]")
#define DS1(x) "v_dot2c_f32_bf16 %[s1], %[one], " x "\n\tv_dot2c_f32_bf16 %[s2], " x ", " x "\n\t"
#define RED2(ctl) "v_add_f32_dpp %[s1], %[s1], %[s1] " ctl " row_mask:0xf bank_mask:0xf\n\tv_add_f32_dpp %[s2], %[s2], %[s2] " ctl " row_mask:0xf bank_mask:0xf\n\t"
#define SWP2(op) "v_mov_b32 %[t1], %[s1]\n\tv_mov_b32 %[t2], %[s2]\n\ts_nop 1\n\t" op " %[s1], %[t1]\n\t" op " %[s2], %[t2]\n\tv_add_f32 %[s1], %[s1], %[t1]\n\tv_add_f32 %[s2], %[s2], %[t2]\n\t"
#define Z32_CLOBBER "v96", "v97", "v98", "v99", "v100", "v101", "v102", "v103", "v104", "v105", "v106", "v107", "v108", "v109", "v110", "v111", \
                    "v112", "v113", "v114", "v115", "v116", "v117", "v118", "v119", TU_CLOBBER
#define Z28_CLOBBER "v96", "v97", "v98", "v99", "v104", "v105", "v106", "v107", "v108", "v109", "v110", "v111", \
                    "v112", "v113", "v114", "v115", "v116", "v117", "v118", "v119", TU_CLOBBER
#define RT "v[120:123]"
#define RU "v[124:127]"
#define TU_CLOBBER "v120", "v121", "v122", "v123", "v124", "v125", "v126", "v127"

__global__ void __launch_bounds__(NT, 2) fused_decode(Args a_) {
  extern __shared__ __attribute__((aligned(16))) unsigned char smem_[];
  LAS unsigned char* const L = (LAS unsigned char*)smem_;
  const int lane_ = threadIdx.x & 63;
  const int w_ = __builtin_amdgcn_readfirstlane(threadIdx.x >> 6);
  const int c_ = blockIdx.x;
  const kargs_t ka_ = (kargs_t)__builtin_amdgcn_kernarg_segment_ptr();
  v4u kr[16], vr[16];
#define LDK(i) asm volatile("buffer_load_dwordx4 %0, %1, %2, 0 offen offset:%3 nt" : "+a"(kr[i]) : "v"((((i) >> 1) & 1) ? voK1 : voK0), "s"(rs_k), "n"(((i) >> 2) * 256 + ((i) & 1) * 128) : "memory")
  int c_late = c_;
  {
    u64 kp, wp;
    asm volatile("s_load_dwordx2 %0, %2, 0x8\n\ts_load_dwordx2 %1, %2, 0x48" : "=&s"(kp), "=&s"(wp) : "s"(ka_));
    const int g = c_ & 7, r = (c_ >> 3) ^ RX, h = 4 * g + (r >> 3), b = (r & 7) >> 1, half = r & 1;
    const u32 off = (u32)((b * 32 + h) * SK + (SK - WIN) + half * 512 + w_ * 64) * (DH * 2);
    const u32 voK0 = (lane_ & 7) * 1024 + ((lane_ >> 3) & 1) * 64 + (lane_ >> 4) * 16;
#pragma unroll
    for (int i = 0; i < 16; ++i) { asm volatile("" : "=a"(kr[i])); asm volatile("" : "=v"(vr[i])); }
    asm volatile("s_waitcnt lgkmcnt(0)" : "+s"(kp), "+s"(wp));
    if (w_ == 3) {
      const rsrc_t rs = mk_rsrc((const void*)wp, WS_BYTES);
      const v4u sv = {SENT, SENT, SENT, SENT};
      v2u gen;
      asm volatile("s_nop 4\n\tbuffer_load_dwordx2 %0, %1, %2, 0 offen sc1" : "=v"(gen) : "v"(WS_G), "s"(rs) : "memory");
      st16_sc1(sv, WS_S + c_ * 2560 + lane_ * 16, rs);
      st16_sc1(sv, WS_S + c_ * 2560 + 1024 + lane_ * 16, rs);
      st16_sc1(sv, lane_ < 32 ? WS_S + c_ * 2560 + 2048 + lane_ * 16 : OOB, rs);
      asm volatile("s_waitcnt vmcnt(0)" : "+v"(gen)::"memory");
      const u64 tag = (((u64)gen.y << 32) | gen.x) + 1ull;
      st8_sc1(v2u{(u32)tag, (u32)(tag >> 32)}, lane_ == 0 ? WS_FLAG + c_ * 8 : OOB, rs);
    }
    const rsrc_t rs_k = mk_rsrc((const void*)(kp + off), 16384);
    asm volatile("s_nop 4\n\tbuffer_load_dwordx4 %0, %2, %3, 0 offen offset:0 nt\n\tbuffer_load_dwordx4 %1, %2, %3, 0 offen offset:128 nt"
                 : "+a"(kr[0]), "+a"(kr[1]) : "v"(voK0), "s"(rs_k) : "memory");
    LAUNDER_S(c_late);
  }
  const int role = ((c_late & 7) < 4 && (c_late >> 3) < 20) ? 3 : (((c_late & 7) == 4 || (c_late & 7) == 5) && (c_late >> 3) < 27) ? 4 : ((c_late & 7) >= 6 && (c_late >> 3) < 20) ? 5 : 0;
  const rsrc_t rs_ws = mk_rsrc(a_.ws, WS_BYTES);
  u16* const outp = a_.out;

  v4u xa;
  u32 tag_lo, tag_hi;
  rsrc_t rs_wr;
  u32 wr_lane, wr_lane1, wr_step, wr_lds;
#define ROLE_DMA(i) dma16(wr_lds + (i) * 1024, ((i) & 1) ? wr_lane1 : wr_lane, rs_wr, (i) * wr_step)
#define NE3 6
#define NE 6
#define NE5 2
#define EARLY_DMA(i) do { if (it == 0 && (((i) < NE3 && role == 3) || ((i) < NE && role == 4) || ((i) < NE5 && role == 5))) ROLE_DMA(i); } while (0)
#define WAIT_EARLY(base, n) do { if (role == 0) WAITVM_I(base); else if (role == 3) WAITVM_I((base) + (NE3 < (n) ? NE3 : (n))); \
    else if (role == 4) WAITVM_I((base) + (NE < (n) ? NE : (n))); else WAITVM_I((base) + (NE5 < (n) ? NE5 : (n))); } while (0)
#define KV_DESC() const size_t kvoff = ((size_t)(b * 32 + h) * SK + (SK - WIN) + half * 512 + w * 64) * DH; \
  const rsrc_t rs_k = mk_rsrc(ka->k + kvoff, 16384), rs_v = mk_rsrc(ka->v + kvoff, 16384); (void)rs_v; \
  const u32 voK0 = (lane & 7) * 1024 + ((lane >> 3) & 1) * 64 + g4 * 16, voK1 = voK0 + 8192, voV = lane * 16; (void)voV
#define LDV(i) asm volatile("buffer_load_dwordx4 %0, %1, %2, %3 offen nt" : "+v"(vr[i]) : "v"(voV), "s"(rs_v), "s"((u32)((i) * 1024)) : "memory")
#define DMA_LANE(stride) ((u32)(lane >> 2) * (stride) + (u32)(((lane & 3) ^ (((lane >> 5) & 1) << 1)) * 16))
#define WF0() ((u32)((8 * g4 + (c16 >> 2)) * 64 + ((((c16 & 3) >> 1) ^ ((g4 & 1) << 1)) * 16) + (c16 & 1) * 8))

  {
    STAGE_VARS();
    KV_DESC();
    if (w == 3 && lane == 0) *(LAS v4u*)(L + L_ZERO) = v4u{0, 0, 0, 0};
    if (w == 2 || (w == 3 && lane < 20)) *(LAS v4u*)(L + L_ONE + ((w - 2) * 64 + lane) * 16) = v4u{ONES, ONES, ONES, ONES};
    v2u gen;
    asm volatile("buffer_load_dwordx2 %0, %1, %2, 0 offen sc1" : "=v"(gen) : "v"(WS_G), "s"(rs_ws) : "memory");
    const rsrc_t rs_x = mk_rsrc(ka->x, 4 * DM * 2);
    const int kq = bh & 3, nh = bh >> 2;
    const u32 u = w * 64 + lane;
    asm volatile("s_nop 4\n\tbuffer_load_dwordx4 %0, %1, %2, 0 offen"
                 : "=&v"(xa) : "v"(w < 5 ? ((u >> 1) & 3) * 1280 + (u >> 3) * 32 + (u & 1) * 16 : OOB), "s"(rs_x) : "memory");
    const rsrc_t rs_wq = mk_rsrc(ka->wq + (size_t)(160 * kq) * 4096 + h * 128 + 64 * nh, 160 * 8192);
    const u32 vo_wq = (lane >> 3) * 8192 + ((((((lane & 7) >> 1) ^ (((lane >> 4) & 1) | ((w & 1) << 1))) << 1) | (lane & 1)) * 16);
#define WQ_DMA(p) dma16(L_WQ + (p) * 1024, vo_wq, rs_wq, (p) * 65536)
    {
      const int s0 = w < 6 ? 7 * w : 42 + 6 * (w - 6), j4 = (g - 4) * 27 + r;
      const int e8 = 2 * g + (r >= 10 ? 1 : 0), n10 = r >= 10 ? r - 10 : r;
      const u16* base = role == 3 ? ka->wo + (size_t)(512 * e8 + 64 * w) * DM + 64 * n10
                      : role == 4 ? ((w & 1) ? ka->wu : ka->wg) + (size_t)(160 * (w >> 1)) * FF + 32 * j4
                                  : ka->wd + (size_t)(32 * s0) * DM + 16 * (role == 5 ? (g - 6) * 20 + r : 0);
      const u32 stride = role == 4 ? 3456 : 1280;
      const u32 rows = role == 3 ? 64 : role == 4 ? 160 : role == 5 ? (w < 6 ? 224 : 192) : 0;
      rs_wr = mk_rsrc(base, rows * stride);
      wr_lane = role == 3 ? (lane >> 3) * 1280 + ((((((lane & 7) >> 1) ^ ((lane >> 4) & 1)) << 1) | (lane & 1)) * 16)
              : role == 4 ? DMA_LANE(3456) : ((lane >> 1) ^ (((lane >> 4) & 1) << 2)) * 1280 + (lane & 1) * 16;
      wr_lane1 = role == 3 ? wr_lane ^ 64 : wr_lane;
      wr_step = role == 3 ? 8 * 1280 : role == 4 ? 16 * 3456 : 32 * 1280;
      wr_lds = role == 3 ? L_WR + w * 8192 : role == 4 ? L_WR + (w & 1) * 40960 + (w >> 1) * 10240 : role == 5 ? L_WR + s0 * 1024 : L_WR + w * 8192;
    }
    asm volatile("s_nop 4" ::: "memory");
#define WP 4
#define WP3 6
#define WP47 WP
#define THP() do { if (w == 3) WAITVM_(WP3); else if (w >= 4) WAITVM_(WP47); else WAITVM_(WP); } while (0)
    LDK(2); LDK(3); WQ_DMA(w);
    THP(); LDK(4); THP(); LDK(5); THP(); LDK(6); THP(); LDK(7); THP(); WQ_DMA(w + 8);
    THP();
    asm volatile("" : "+v"(gen), "+v"(xa));
    const u64 tag64 = (((u64)(u32)__builtin_amdgcn_readfirstlane(gen.y) << 32) | (u32)__builtin_amdgcn_readfirstlane(gen.x)) + 1ull;
    tag_lo = (u32)tag64; tag_hi = (u32)(tag64 >> 32);
    LDK(8); THP(); LDK(9); THP(); LDK(10); THP(); LDK(11);
    if (w >= 4) { THP(); WQ_DMA(w + 12); }
    THP(); LDK(12); THP(); LDK(13); THP(); LDK(14); THP(); LDK(15);
    WAITVM(4);
    lds_barrier();
  }

#pragma unroll 1
  for (int it = 0; it < 2; ++it) {
    {
      STAGE_VARS();
      const int kq = bh & 3;
      v4u wqf[5];
      if (w >= 4) {
        const u32 q = c16 >> 2, swz = ((q >> 1) & 1) | ((g4 & 1) << 1);
        const u32 wf0 = L_WQ + (8 * g4 + q) * 128 + (((w - 4) ^ swz) << 5) + (c16 & 3) * 8;
#pragma unroll
        for (int s = 0; s < 5; ++s) wqf[s] = wfrag<128>(L + wf0 + s * 4096);
#pragma unroll
        for (int i = 0; i < 5; ++i) asm volatile("" : "+a"(wqf[i]));
      } else {
#pragma unroll
        for (int i = 0; i < 5; ++i) asm volatile("" : "=a"(wqf[i]));
      }
      const u32 u = w * 64 + lane;
      u32 rl = L_RQ + (c16 >> 2) * 256 + g4 * 64 + (c16 & 3) * 16, csr = L_CS + c16 * 16;
      u32 g2r = L_GR + g4 * 20, g1r = L_GR + 64 + g4 * 16;
      u32 qout = it == 0 ? WS_Q1 + c * 1024 + lane * 16 : ((lane & 1) ? OOB : S_Q2 + (h * 8 + bh) * 512 + lane * 8);
      u32 xsw = L_XS + (u >> 4) * 320 + ((u >> 1) & 3) * 64 + ((u >> 3) & 1) * 32 + (u & 1) * 16;
      u32 xfr = w < 4 ? (c16 < 4 ? L_XS + 5 * w * 320 + c16 * 64 : L_ONE) + g4 * 16 : L_XS + 5 * kq * 320 + (c16 & 3) * 64 + g4 * 16;
      u32 lw = w < 4 ? L_GR + w * 128 + ((g4 & 1) * 4 + (c16 & 3)) * 16 : L_RQ + (w - 4) * 256 + c16 * 64 + g4 * 16;
      u32 o1 = OOB;
      if (it == 0) {
        if (w >= 4) {
          const v4u one = {ONES, ONES, ONES, ONES};
          f4 c0;
          asm volatile("s_nop 1\n\t" MF("%0", "%1", "%6", "0") MF("%0", "%2", "%6", "%0") MF("%0", "%3", "%6", "%0") MF("%0", "%4", "%6", "%0") MF("%0", "%5", "%6", "%0") "s_nop 7"
                       : "=&v"(c0) : "a"(wqf[0]), "a"(wqf[1]), "a"(wqf[2]), "a"(wqf[3]), "a"(wqf[4]), "v"(one));
          if (c16 == 0) *(LAS f4*)(L + L_CS + (w - 4) * 64 + g4 * 16) = c0;
        }
      } else {
        if (w < 5) o1 = S_X2 + g * 5120 + u * 16;
        if (role == 0) idle<SLP_X_NONE>(); else if (role == 3) idle<SLP_X_S3>(); else if (role == 4) idle<SLP_X_S4>();
      }
      LAUNDER_V(rl); LAUNDER_V(csr); LAUNDER_V(g2r); LAUNDER_V(g1r); LAUNDER_V(qout); LAUNDER_V(xsw); LAUNDER_V(xfr); LAUNDER_V(lw); LAUNDER_V(o1);
      u32 cnt = SPIN_MAX;
#define Q_HEAD "s_cmp_gt_u32 %[w], 4\n\ts_cbranch_scc1 .Ln%=\n\ts_cmp_eq_u32 %[it], 0\n\ts_cbranch_scc1 .Ld%=\n" A_POLL \
            "buffer_load_dwordx4 v[96:99], %[o1], %[rs], 0 offen sc1\n\ts_waitcnt vmcnt(0)\n\tv_max3_u32 v104, v96, v97, v98\n\tv_max_u32 v104, v104, v99\n\t" \
            A_DONE("v104") "ds_write_b128 %[xsw], v[96:99]\n.Ln%=:\n\t" A_BAR "\n\t" \
            "ds_read_b128 v[104:107], %[xfr]\n\tds_read_b128 v[108:111], %[xfr] offset:320\n\tds_read_b128 v[112:115], %[xfr] offset:640\n\t" \
            "ds_read_b128 v[116:119], %[xfr] offset:960\n\tds_read_b128 v[120:123], %[xfr] offset:1280\n\t"
      if (w < 4) {
        asm volatile(Q_HEAD
            "s_waitcnt lgkmcnt(4)\n\t" MF("v[100:103]", "v[104:107]", "v[104:107]", "0") "s_waitcnt lgkmcnt(3)\n\t" MF("v[100:103]", "v[108:111]", "v[108:111]", "v[100:103]")
            "s_waitcnt lgkmcnt(2)\n\t" MF("v[100:103]", "v[112:115]", "v[112:115]", "v[100:103]") "s_waitcnt lgkmcnt(1)\n\t" MF("v[100:103]", "v[116:119]", "v[116:119]", "v[100:103]")
            "s_waitcnt lgkmcnt(0)\n\t" MF("v[100:103]", "v[120:123]", "v[120:123]", "v[100:103]")
 "s_mov_b32 exec_lo, 0x000f000f\n\ts_mov_b32 exec_hi, 0\n\ts_nop 5\n\tds_write_b128 %[lw], v[100:103]\n\t" A_EXEC_ALL A_BAR
            : "+{v[96:99]}"(xa), [cnt] "+s"(cnt)
            : [o1] "v"(o1), [rs] "s"(rs_ws), [it] "s"(it), [w] "s"(w), [xsw] "v"(xsw), [xfr] "v"(xfr), [lw] "v"(lw)
            : "v100", "v101", "v102", "v103", "v104", "v105", "v106", "v107", "v108", "v109", "v110", "v111", "v112", "v113", "v114", "v115",
              "v116", "v117", "v118", "v119", "v120", "v121", "v122", "v123", "vcc", "scc", "memory");
      } else {
        asm volatile(Q_HEAD
            "s_waitcnt lgkmcnt(4)\n\t" MF("v[100:103]", "%[w0]", "v[104:107]", "0") "s_waitcnt lgkmcnt(3)\n\t" MF("v[100:103]", "%[w1]", "v[108:111]", "v[100:103]")
            "s_waitcnt lgkmcnt(2)\n\t" MF("v[100:103]", "%[w2]", "v[112:115]", "v[100:103]") "s_waitcnt lgkmcnt(1)\n\t" MF("v[100:103]", "%[w3]", "v[116:119]", "v[100:103]")
            "s_waitcnt lgkmcnt(0)\n\t" MF("v[100:103]", "%[w4]", "v[120:123]", "v[100:103]")
 A_EXEC_C4 "s_nop 5\n\tds_write_b128 %[lw], v[100:103]\n\t" A_EXEC_ALL A_BAR
            : "+{v[96:99]}"(xa), [cnt] "+s"(cnt)
            : [o1] "v"(o1), [rs] "s"(rs_ws), [it] "s"(it), [w] "s"(w), [xsw] "v"(xsw), [xfr] "v"(xfr), [lw] "v"(lw),
              [w0] "a"(wqf[0]), [w1] "a"(wqf[1]), [w2] "a"(wqf[2]), [w3] "a"(wqf[3]), [w4] "a"(wqf[4])
            : "v100", "v101", "v102", "v103", "v104", "v105", "v106", "v107", "v108", "v109", "v110", "v111", "v112", "v113", "v114", "v115",
              "v116", "v117", "v118", "v119", "v120", "v121", "v122", "v123", "vcc", "scc", "memory");
      }
      xa = v4u{0, 0, 0, 0};
      if (w == 0) {
        u32 d0, d1;
        asm volatile(
            "ds_read2_b32 v[108:109], %[g1r] offset0:0 offset1:32\n\tds_read2_b32 v[110:111], %[g1r] offset0:64 offset1:96\n\t"
            "ds_read2_b32 v[112:113], %[g2r] offset0:0 offset1:32\n\tds_read2_b32 v[114:115], %[g2r] offset0:64 offset1:96\n\t"
            "ds_read_b128 v[96:99], %[rl]\n\tds_read_b128 v[100:103], %[csr]\n\t"
            "s_waitcnt lgkmcnt(4)\n\t" A_PKADD("v[108:109]", "v[108:109]", "v[110:111]")
            "s_waitcnt lgkmcnt(2)\n\t" A_PKADD("v[112:113]", "v[112:113]", "v[114:115]")
            "v_add_f32 v104, v108, v109\n\tv_add_f32 v105, v112, v113\n\tv_mul_f32 v104, 0x3acccccd, v104\n\tv_mul_f32 v105, 0x3acccccd, v105\n\t"
            "v_fma_f32 v105, -v104, v104, v105\n\tv_max_f32 v105, 0, v105\n\tv_add_f32 v105, 0x3727c5ac, v105\n\tv_rsq_f32 v105, v105\n\ts_waitcnt lgkmcnt(0)\n\t"
            "v_pk_fma_f32 v[96:97], v[100:101], v[104:105], v[96:97] op_sel_hi:[1,0,1] neg_lo:[0,1,0] neg_hi:[0,1,0]\n\t"
            "v_pk_fma_f32 v[98:99], v[102:103], v[104:105], v[98:99] op_sel_hi:[1,0,1] neg_lo:[0,1,0] neg_hi:[0,1,0]\n\t"
            "v_pk_mul_f32 v[96:97], v[96:97], v[104:105] op_sel:[0,1] op_sel_hi:[1,1]\n\t"
            "v_pk_mul_f32 v[98:99], v[98:99], v[104:105] op_sel:[0,1] op_sel_hi:[1,1]\n\t"
            "v_cvt_pk_bf16_f32 %[d0], v96, v97\n\tv_cvt_pk_bf16_f32 %[d1], v98, v99\n\tv_min_u32 %[d0], -2, %[d0]\n\tv_min_u32 %[d1], -2, %[d1]\n\ts_nop 1"
            : [d0] "=&v"(d0), [d1] "=&v"(d1) : [rl] "v"(rl), [g1r] "v"(g1r), [g2r] "v"(g2r), [csr] "v"(csr)
            : "v96", "v97", "v98", "v99", "v100", "v101", "v102", "v103", "v104", "v105", "v108", "v109", "v110", "v111", "v112", "v113", "v114", "v115", "memory");
        if (it == 0) st16_sc1(v4u{d0, d1, tag_lo, tag_hi}, qout, rs_ws);
        else st16_sc1(v4u{d0, d1, dppu<SHL1>(d0), dppu<SHL1>(d1)}, qout, rs_ws);
      }
    }
    v4u qv;
    if (it == 0) {
      STAGE_VARS();
      KV_DESC();
      asm volatile("s_nop 4" ::: "memory");
      const u32 q1off = WS_Q1 + (g + 8 * (((h & 3) * 8 + (c16 >> 3) * 4 + g4) ^ RX)) * 1024 + b * 256 + (c16 & 7) * 32;
      v4u p1, p2, fl0, fl1;
#define W3 4
#define WN 3
#define WN47 WN
#define THR() do { if (w >= 4) WAITVM_(WN47); else WAITVM_(WN); } while (0)
#define KD(i) THR(); LDK(8 + i);
      THR(); LDV(0); THR(); LDV(1); THR(); LDV(2); THR(); LDV(3); THR(); LDV(4); THR(); LDV(5); THR(); LDV(6); THR(); LDV(7);
      asm volatile("buffer_load_dwordx4 %0, %2, %3, 0 offen sc1\n\tbuffer_load_dwordx4 %1, %2, %3, 0 offen offset:1024 sc1"
                   : "=&v"(fl0), "=&v"(fl1) : "v"(w == 7 ? WS_FLAG + lane * 16 : OOB), "s"(rs_ws) : "memory");
#define WT 3
#define THT() WAITVM_(WT)
      THT(); LDV(8); THT(); LDV(9);
      asm volatile("buffer_load_dwordx4 %0, %2, %3, 0 offen sc1\n\tbuffer_load_dwordx4 %1, %2, %3, 0 offen offset:16 sc1" : "=&v"(p1), "=&v"(p2) : "v"(q1off), "s"(rs_ws) : "memory");
      THT(); LDV(10); THT(); LDV(11); THT(); LDV(12); THT(); LDV(13); THT(); LDV(14);
      THT(); LDV(15);
      if (WT > 3) {   }
      if (WT > 3) WAITVM(4);
      EARLY_DMA(0);
      asm volatile("" : "+v"(p1), "+v"(p2), "+v"(fl0), "+v"(fl1));
      _Pragma("nounroll") for (u32 spin = 0; spin < SPIN_MAX; ++spin) {
        if (__builtin_amdgcn_ballot_w64(p1.z != tag_lo || p1.w != tag_hi || p2.z != tag_lo || p2.w != tag_hi) == 0ull) break;
        asm volatile("buffer_load_dwordx4 %0, %2, %3, 0 offen sc1\n\tbuffer_load_dwordx4 %1, %2, %3, 0 offen offset:16 sc1\n\ts_waitcnt vmcnt(0)"
                     : "=&v"(p1), "=&v"(p2) : "v"(q1off), "s"(rs_ws) : "memory");
      }
      qv = v4u{p1.x, p1.y, p2.x, p2.y};
      if (w == 7) {
        _Pragma("nounroll") for (u32 spin = 0; spin < SPIN_MAX; ++spin) {
          const bool bad = fl0.x != tag_lo || fl0.y != tag_hi || fl0.z != tag_lo || fl0.w != tag_hi || fl1.x != tag_lo || fl1.y != tag_hi || fl1.z != tag_lo || fl1.w != tag_hi;
          if (__builtin_amdgcn_ballot_w64(bad) == 0ull) break;
          asm volatile("buffer_load_dwordx4 %0, %2, %3, 0 offen sc1\n\tbuffer_load_dwordx4 %1, %2, %3, 0 offen offset:1024 sc1\n\ts_waitcnt vmcnt(0)"
                       : "=&v"(fl0), "=&v"(fl1) : "v"(WS_FLAG + lane * 16), "s"(rs_ws) : "memory");
        }
      }
    } else {
      qv = v4u{0, 0, 0, 0};
    }
    const u32 pbase = it * PASS_STRIDE;
    {
      STAGE_VARS();
      u32 qoff = S_Q2 + (h * 8 + (c16 >> 3) * 4 + g4) * 512 + b * 128 + (c16 & 7) * 16;
      const u32 qb = L_QB + (it == 0 ? w * 256 : 0);
      u32 qbw = qb + c16 * 16 + g4 * 8, qbr = qb + (c16 & 1) * 64 + g4 * 16;
      u32 raw = L_RA + w * 512 + c16 * 32 + g4 * 8, rsw = L_RA + 4096 + w * 8;
      u32 rar = L_RA + lane * 8, rsr = L_RA + 4096 + (lane & 7) * 8;
      const u32 oslot = S_O0 + pbase + ((h * 2 + half) * 4 + b) * O_SLOT;
      u32 oout = (lane & 3) ? OOB : oslot + lane * 4, sout = lane == 0 ? oslot + 256 : OOB;
      LAUNDER_V(qoff); LAUNDER_V(qbw); LAUNDER_V(qbr); LAUNDER_V(raw); LAUNDER_V(rsw); LAUNDER_V(rar); LAUNDER_V(rsr); LAUNDER_V(oout); LAUNDER_V(sout);
      if (w < 4) __builtin_amdgcn_s_setprio(1);
      if (it == 1) idle<SLP_Q>();
      float sraw;
      {
        float sb;
        u32 cnt = SPIN_MAX;
        v4u qsel = sum_selector(c16);
#define SEL(d, s, rm, bm) "v_mov_b32_dpp " d ", " s " quad_perm:[0,1,2,3] row_mask:" rm " bank_mask:" bm "\n\t"
#define QKMF0(d, k) "v_mfma_f32_16x16x32_bf16 " d ", v[88:91], %[" k "], 0\n\t"
#define QKMF1(d, k) "v_mfma_f32_16x16x32_bf16 " d ", v[92:95], %[" k "], " d "\n\t"
        asm volatile( "s_cmp_eq_u32 %[it], 0\n\ts_cbranch_scc1 .Ld%=\n\ts_cmp_lg_u32 %[w], 0\n\ts_cbranch_scc1 .Lw%=\n" A_POLL
            "buffer_load_dwordx4 v[96:99], %[qo], %[rs], 0 offen sc1\n\ts_waitcnt vmcnt(0)\n\tv_max3_u32 v100, v96, v97, v98\n\tv_max_u32 v100, v100, v99\n\t"
            A_DONE("v100")
            "s_nop 1\n\tv_mfma_f32_16x16x32_bf16 v[100:103], v[104:107], v[96:99], 0\n\ts_mov_b32 exec_hi, 0\n\ts_nop 6\n\t"
            "v_cvt_pk_bf16_f32 v96, v100, v101\n\tv_cvt_pk_bf16_f32 v97, v102, v103\n\tds_write_b64 %[qbw], v[96:97]\n\ts_mov_b64 exec, -1\n"
            ".Lw%=:\n\ts_waitcnt lgkmcnt(0)\n\ts_cmp_eq_u32 %[it], 0\n\ts_cbranch_scc1 .Lr%=\n\ts_barrier\n.Lr%=:\n\t"
            "ds_read_b128 v[88:91], %[qbr]\n\tds_read_b128 v[92:95], %[qbr] offset:128\n\ts_waitcnt lgkmcnt(1)\n\t"
            QKMF0("v[96:99]", "k0") QKMF0("v[100:103]", "k2") QKMF0("v[104:107]", "k4") QKMF0("v[108:111]", "k6")
            QKMF0("v[112:115]", "k8") QKMF0("v[116:119]", "k10") QKMF0("v[120:123]", "k12") QKMF0("v[124:127]", "k14")
            "s_waitcnt lgkmcnt(0)\n\t"
            QKMF1("v[96:99]", "k1") QKMF1("v[100:103]", "k3") QKMF1("v[104:107]", "k5") QKMF1("v[108:111]", "k7")
            QKMF1("v[112:115]", "k9") QKMF1("v[116:119]", "k11") QKMF1("v[120:123]", "k13") QKMF1("v[124:127]", "k15")
            "s_nop 0\n\tv_mov_b32 %[sa], v96\n\tv_mov_b32 %[sb], v100\n\t"
            SEL("%[sa]", "v101", "0x1", "0xc") SEL("%[sb]", "v97", "0x1", "0xc")
            SEL("%[sa]", "v104", "0x2", "0x3") SEL("%[sb]", "v108", "0x2", "0x3") SEL("%[sa]", "v109", "0x2", "0xc") SEL("%[sb]", "v105", "0x2", "0xc")
            SEL("%[sa]", "v112", "0x4", "0x3") SEL("%[sb]", "v116", "0x4", "0x3") SEL("%[sa]", "v117", "0x4", "0xc") SEL("%[sb]", "v113", "0x4", "0xc")
            SEL("%[sa]", "v120", "0x8", "0x3") SEL("%[sb]", "v124", "0x8", "0x3") SEL("%[sa]", "v125", "0x8", "0xc") SEL("%[sb]", "v121", "0x8", "0xc")
            "s_nop 1\n\tv_add_f32_dpp %[sa], %[sb], %[sa] row_ror:8 row_mask:0xf bank_mask:0xf"
            : "+{v[96:99]}"(qv), "+{v[104:107]}"(qsel), [sa] "=&v"(sraw), [sb] "=&v"(sb), [cnt] "+s"(cnt)
            : [qo] "v"(qoff), [rs] "s"(rs_ws), [it] "s"(it), [w] "s"(w), [qbw] "v"(qbw), [qbr] "v"(qbr),
              [k0] "a"(kr[0]), [k1] "a"(kr[1]), [k2] "a"(kr[2]), [k3] "a"(kr[3]), [k4] "a"(kr[4]), [k5] "a"(kr[5]), [k6] "a"(kr[6]), [k7] "a"(kr[7]),
              [k8] "a"(kr[8]), [k9] "a"(kr[9]), [k10] "a"(kr[10]), [k11] "a"(kr[11]), [k12] "a"(kr[12]), [k13] "a"(kr[13]), [k14] "a"(kr[14]), [k15] "a"(kr[15])
            : "v88", "v89", "v90", "v91", "v92", "v93", "v94", "v95", "v100", "v101", "v102", "v103",
              "v108", "v109", "v110", "v111", "v112", "v113", "v114", "v115", "v116", "v117", "v118", "v119", TU_CLOBBER, "vcc", "scc", "memory");
      }
      EARLY_DMA(1);
      float mw, lw, oc[8];
      u32 pp;
      {
        float t;
#define RMAX(ctl) "v_max_f32_dpp %[m], %[m], %[m] " ctl " row_mask:0xf bank_mask:0xf\n\t"
        asm volatile(
            "v_mul_f32 %[l], 0x3e0293ee, %[s]\n\tv_mov_b32 %[o0], 0\n\tv_mov_b32 %[o1], 0\n\t"
            "v_max_f32_dpp %[m], %[l], %[l] row_ror:8 row_mask:0xf bank_mask:0xf\n\tv_mov_b32 %[o2], 0\n\tv_mov_b32 %[o3], 0\n\t"
            RMAX("row_ror:4") "v_mov_b32 %[o4], 0\n\tv_mov_b32 %[o5], 0\n\t" RMAX("row_ror:2") "v_mov_b32 %[o6], 0\n\tv_mov_b32 %[o7], 0\n\t" RMAX("row_ror:1")
            "v_mov_b32 %[t], %[m]\n\ts_nop 1\n\tv_permlane16_swap_b32 %[m], %[t]\n\tv_max_f32 %[m], %[m], %[t]\n\t"
            "v_mov_b32 %[t], %[m]\n\ts_nop 1\n\tv_permlane32_swap_b32 %[m], %[t]\n\tv_max_f32 %[m], %[m], %[t]\n\t"
            "v_sub_f32 %[l], %[l], %[m]\n\tv_exp_f32 %[l], %[l]\n\ts_nop 1\n\t"
            "v_mov_b32_dpp %[t], %[l] row_shl:1 row_mask:0xf bank_mask:0xf bound_ctrl:1\n\tv_cvt_pk_bf16_f32 %[p], %[l], %[t]"
            : [m] "=&v"(mw), [l] "=&v"(lw), [p] "=&v"(pp), [t] "=&v"(t), [o0] "=&v"(oc[0]), [o1] "=&v"(oc[1]), [o2] "=&v"(oc[2]), [o3] "=&v"(oc[3]),
              [o4] "=&v"(oc[4]), [o5] "=&v"(oc[5]), [o6] "=&v"(oc[6]), [o7] "=&v"(oc[7])
            : [s] "v"(sraw));
      }
      EARLY_DMA(2);
#define VP1(a, b) "v_perm_b32 %8, " b ", " a ", %9\n\tv_perm_b32 " b ", " b ", " a ", %10\n\tv_mov_b32 " a ", %8\n\t"
#define VPAIR(m) do { u32 t_; asm volatile(VP1("%0", "%4") VP1("%1", "%5") VP1("%2", "%6") VP1("%3", "%7") \
      : "+v"(vr[2 * (m)][0]), "+v"(vr[2 * (m)][1]), "+v"(vr[2 * (m)][2]), "+v"(vr[2 * (m)][3]), \
        "+v"(vr[2 * (m) + 1][0]), "+v"(vr[2 * (m) + 1][1]), "+v"(vr[2 * (m) + 1][2]), "+v"(vr[2 * (m) + 1][3]), "=&v"(t_) \
      : "s"(0x05040100u), "s"(0x07060302u)); } while (0)
#define D2(d, v, bc) "v_dot2c_f32_bf16_dpp " d ", %10, " v " row_newbcast:" #bc " row_mask:0xf bank_mask:0xf\n\t"
#define LSUM(ctl) "v_add_f32_dpp %8, %8, %8 " ctl " row_mask:0xf bank_mask:0xf"
#define LSWP(op) "v_mov_b32 %9, %8\n\ts_nop 1\n\t" op " %8, %9\n\tv_add_f32 %8, %8, %9"
#define PV(m, bc, pre, post) do { float t_; asm volatile(pre D2("%0", "%11", bc) D2("%1", "%15", bc) D2("%2", "%12", bc) D2("%3", "%16", bc) \
      D2("%4", "%13", bc) D2("%5", "%17", bc) D2("%6", "%14", bc) D2("%7", "%18", bc) post \
      : "+v"(oc[0]), "+v"(oc[1]), "+v"(oc[2]), "+v"(oc[3]), "+v"(oc[4]), "+v"(oc[5]), "+v"(oc[6]), "+v"(oc[7]), "+v"(lw), "=&v"(t_) \
      : "v"(pp), "v"(vr[2 * (m)][0]), "v"(vr[2 * (m)][1]), "v"(vr[2 * (m)][2]), "v"(vr[2 * (m)][3]), \
        "v"(vr[2 * (m) + 1][0]), "v"(vr[2 * (m) + 1][1]), "v"(vr[2 * (m) + 1][2]), "v"(vr[2 * (m) + 1][3])); } while (0)
      if (it == 0) {
#pragma unroll
        for (int i = 0; i < 12; ++i) asm volatile("" : "+v"(vr[i]));
        VPAIR(0); VPAIR(1); VPAIR(2); VPAIR(3); VPAIR(4); VPAIR(5);
      }
      EARLY_DMA(3);
      PV(0, 0, "s_nop 1\n\t", LSUM("row_ror:8")); PV(1, 2, "", LSUM("row_ror:4"));
      EARLY_DMA(4);
      PV(2, 4, "", LSUM("row_ror:2")); PV(3, 6, "", LSUM("row_ror:1"));
      EARLY_DMA(5);
      PV(4, 8, "", LSWP("v_permlane16_swap_b32")); PV(5, 10, "", LSWP("v_permlane32_swap_b32"));
      EARLY_DMA(6);
      if (it == 0) {
        WAIT_EARLY(2, 7);
        asm volatile("" : "+v"(vr[12]), "+v"(vr[13]));
        VPAIR(6);
      }
      PV(6, 12, "", "");
      EARLY_DMA(7);
      if (it == 0) {
        WAIT_EARLY(0, 8);
        asm volatile("" : "+v"(vr[14]), "+v"(vr[15]));
        VPAIR(7);
      }
      PV(7, 14, "", "s_nop 2");
      asm volatile(
          "v_permlane32_swap_b32 %0, %4\n\tv_permlane32_swap_b32 %1, %5\n\tv_permlane32_swap_b32 %2, %6\n\tv_permlane32_swap_b32 %3, %7\n\t"
          "v_add_f32 %0, %0, %4\n\tv_add_f32 %2, %2, %6\n\tv_add_f32 %1, %1, %5\n\tv_add_f32 %3, %3, %7\n\t"
          "v_mov_b32 v98, %8\n\tv_permlane16_swap_b32 %0, %2\n\tv_mov_b32 v99, %9\n\tv_permlane16_swap_b32 %1, %3\n\t"
          "v_add_f32 v96, %0, %2\n\tv_add_f32 v97, %1, %3\n\tds_write_b64 %10, v[96:97]\n\t"
          "s_mov_b64 exec, 1\n\tds_write_b64 %11, v[98:99]\n\ts_mov_b64 exec, -1\n\ts_setprio 0\n\t" A_BAR
          : "+v"(oc[0]), "+v"(oc[1]), "+v"(oc[2]), "+v"(oc[3]), "+v"(oc[4]), "+v"(oc[5]), "+v"(oc[6]), "+v"(oc[7])
          : "v"(mw), "v"(lw), "v"(raw), "v"(rsw) : "v96", "v97", "v98", "v99", "memory");
      if (w == 0) {
        v4u q;
        v2u st;
#define MRG(i, lo, hi) "v_fmac_f32_dpp v96, v120, " lo " row_newbcast:" #i " row_mask:0xf bank_mask:0xf\n\tv_fmac_f32_dpp v97, v120, " hi " row_newbcast:" #i " row_mask:0xf bank_mask:0xf\n\t"
        asm volatile(
            "ds_read_b64 v[118:119], %[rsr]\n\t"
            "ds_read2st64_b64 v[100:103], %[rar] offset0:0 offset1:1\n\tds_read2st64_b64 v[104:107], %[rar] offset0:2 offset1:3\n\t"
            "ds_read2st64_b64 v[108:111], %[rar] offset0:4 offset1:5\n\tds_read2st64_b64 v[112:115], %[rar] offset0:6 offset1:7\n\t"
            "v_mov_b32 v96, 0\n\tv_mov_b32 v97, 0\n\ts_waitcnt lgkmcnt(4)\n\t"
            "v_max_f32_dpp v116, v118, v118 quad_perm:[1,0,3,2] row_mask:0xf bank_mask:0xf\n\ts_nop 1\n\t"
            "v_max_f32_dpp v116, v116, v116 quad_perm:[2,3,0,1] row_mask:0xf bank_mask:0xf\n\ts_nop 1\n\t"
            "v_max_f32_dpp v116, v116, v116 row_half_mirror row_mask:0xf bank_mask:0xf\n\t"
            "v_sub_f32 v120, v118, v116\n\tv_exp_f32 v120, v120\n\ts_nop 0\n\tv_mul_f32 v117, v120, v119\n\ts_nop 1\n\t"
            "v_add_f32_dpp v117, v117, v117 quad_perm:[1,0,3,2] row_mask:0xf bank_mask:0xf\n\ts_nop 1\n\t"
            "v_add_f32_dpp v117, v117, v117 quad_perm:[2,3,0,1] row_mask:0xf bank_mask:0xf\n\ts_nop 1\n\t"
            "v_add_f32_dpp v117, v117, v117 row_half_mirror row_mask:0xf bank_mask:0xf\n\t"
            "v_rcp_f32 v121, v117\n\tv_min_u32 v116, -2, v116\n\tv_min_u32 v117, -2, v117\n\tv_mul_f32 v120, v120, v121\n\ts_waitcnt lgkmcnt(0)\n\ts_nop 0\n\t"
            MRG(0, "v100", "v101") MRG(1, "v102", "v103") MRG(2, "v104", "v105") MRG(3, "v106", "v107")
            MRG(4, "v108", "v109") MRG(5, "v110", "v111") MRG(6, "v112", "v113") MRG(7, "v114", "v115")
            A_PACK_QUAD
            : "=&{v[100:103]}"(q), "=&{v[116:117]}"(st) : [rsr] "v"(rsr), [rar] "v"(rar)
            : "v96", "v97", "v104", "v105", "v106", "v107", "v108", "v109", "v110", "v111", "v112", "v113", "v114", "v115", "v118", "v119", "v120", "v121", "memory");
        st16_sc1(q, oout, rs_ws);
        st8_sc1(st, sout, rs_ws);
      }
    }
    if (role == 3) {
      STAGE_VARS();
      const int e8 = 2 * g + (r >= 10 ? 1 : 0), n10 = r >= 10 ? r - 10 : r;
      const u32 sl = S_O0 + pbase + (((4 * e8 + (w >> 1)) * 2 + (c16 >> 3)) * 4 + (c16 & 3)) * O_SLOT;
      u32 o0off = sl + (w & 1) * 128 + ((c16 >> 2) & 1) * 64 + g4 * 16, stoff = sl + 256;
      if (it == 0) {
        if (NE3 < 8) {
          lds_barrier();
#pragma unroll
          for (int i = NE3; i < 8; ++i) ROLE_DMA(i);
        }
        WAITVM(0);
      }
      v4u wf[8];
      {
        const u32 q = c16 >> 2, swz = ((q >> 1) & 1) | ((g4 & 1) << 1);
        const u32 wf0 = L_WR + w * 8192 + (8 * g4 + q) * 128 + (swz << 5) + (c16 & 3) * 8;
#pragma unroll
        for (int s = 0; s < 2; ++s)
#pragma unroll
          for (int t = 0; t < 4; ++t) wf[s * 4 + t] = wfrag<128>(L + s * 4096 + (wf0 ^ (t * 32)));
      }
      u32 rl = L_RR + (c16 >> 2) * 256 + g4 * 64 + (c16 & 3) * 16, rw = L_RR + w * 1024 + c16 * 64 + g4 * 16;
      u32 xout = S_X1P0 + pbase + (lane & 1) * 40960 + (e8 * 10 + n10) * 512 + g4 * 128 + (c16 & 14) * 8;
      f2 res0 = {0.f, 0.f}, res1 = {0.f, 0.f};
      if (e8 == 0) {
        const v2u x = *(LAS v2u*)(L + L_XS + (2 * n10 + (c16 >> 3)) * 320 + g4 * 64 + (c16 & 7) * 8);
        res0 = f2{bflo(x.x), bfhi(x.x)}; res1 = f2{bflo(x.y), bfhi(x.y)};
      }
#pragma unroll
      for (int i = 0; i < 8; ++i) asm volatile("" : "+a"(wf[i]));
      LAUNDER_V(o0off); LAUNDER_V(stoff); LAUNDER_V(rl); LAUNDER_V(rw); LAUNDER_V(xout); LAUNDER_V(res0); LAUNDER_V(res1);
      if (it == 1) idle<SLP_O>();
      {
        u32 cnt = SPIN_MAX;
#define S3_UNP(lo, hi, d) "v_lshlrev_b32 " lo ", 16, " d "\n\tv_and_b32 " hi ", 0xffff0000, " d "\n\t"
#define S3_SCL(p) "v_pk_mul_f32 " p ", " p ", v[108:109] op_sel_hi:[1,0]\n\t"
#define S3_ADD(r) "v_add_f32_dpp " r ", " r ", " r " row_ror:8 row_mask:0xf bank_mask:0xf\n\t"
        asm volatile( A_POLL
            "buffer_load_dwordx4 v[96:99], %[oo], %[rs], 0 offen sc1\n\tbuffer_load_dwordx2 v[104:105], %[os], %[rs], 0 offen sc1\n\ts_waitcnt vmcnt(0)\n\t"
            "v_max3_u32 v112, v96, v97, v98\n\tv_max3_u32 v113, v99, v104, v105\n\tv_max_u32 v112, v112, v113\n\t"
            A_DONE("v112")
            "v_mov_b32_dpp v106, v104 row_ror:8 row_mask:0xf bank_mask:0xf\n\t"
            S3_UNP("v112", "v113", "v96") "v_max_f32 v106, v104, v106\n\t" S3_UNP("v114", "v115", "v97") "v_sub_f32 v107, v104, v106\n\t"
            S3_UNP("v116", "v117", "v98") "v_exp_f32 v107, v107\n\t" S3_UNP("v118", "v119", "v99") "v_mul_f32 v107, v107, v105\n\ts_nop 1\n\t"
            "v_add_f32_dpp v106, v107, v107 row_ror:8 row_mask:0xf bank_mask:0xf\n\tv_rcp_f32 v106, v106\n\ts_nop 0\n\tv_mul_f32 v108, v107, v106\n\t"
            S3_SCL("v[112:113]") S3_SCL("v[114:115]") S3_SCL("v[116:117]") S3_SCL("v[118:119]")
            S3_ADD("v112") S3_ADD("v113") S3_ADD("v114") S3_ADD("v115") S3_ADD("v116") S3_ADD("v117") "s_nop 0\n\t" S3_ADD("v118") S3_ADD("v119")
            "v_cvt_pk_bf16_f32 v96, v112, v113\n\tv_cvt_pk_bf16_f32 v97, v114, v115\n\tv_cvt_pk_bf16_f32 v98, v116, v117\n\tv_cvt_pk_bf16_f32 v99, v118, v119\n\t"
            "s_nop 1\n\t"
            MF("v[100:103]", "%[w0]", "v[96:99]", "0") MVT("v96", "v97", "v98", "v99", "4") MF("v[104:107]", "%[w1]", "v[96:99]", "0")
            MF("v[108:111]", "%[w2]", "v[96:99]", "0") MF("v[112:115]", "%[w3]", "v[96:99]", "0")
            MF("v[100:103]", "%[w4]", RT, "v[100:103]") MF("v[104:107]", "%[w5]", RT, "v[104:107]") MF("v[108:111]", "%[w6]", RT, "v[108:111]") MF("v[112:115]", "%[w7]", RT, "v[112:115]")
#define S3_MV(d, s, n, m) "v_mov_b32_dpp " d ", " s " row_shr:" n " row_mask:0xf bank_mask:" m "\n\t"
#define S3_MV4(a, b, c, d, n, m) S3_MV("v100", a, n, m) S3_MV("v101", b, n, m) S3_MV("v102", c, n, m) S3_MV("v103", d, n, m)
 "s_nop 5\n\t" S3_MV4("v104", "v105", "v106", "v107", "4", "0x2") S3_MV4("v108", "v109", "v110", "v111", "8", "0x4")
            S3_MV4("v112", "v113", "v114", "v115", "12", "0x8") "ds_write_b128 %[rw], v[100:103]\n\t" A_BAR
            : [cnt] "+s"(cnt)
            : [oo] "v"(o0off), [os] "v"(stoff), [rw] "v"(rw), [rs] "s"(rs_ws),
              [w0] "a"(wf[0]), [w1] "a"(wf[1]), [w2] "a"(wf[2]), [w3] "a"(wf[3]), [w4] "a"(wf[4]), [w5] "a"(wf[5]), [w6] "a"(wf[6]), [w7] "a"(wf[7])
            : Z32_CLOBBER, "vcc", "scc", "memory");
      }
      if (w == 0) {
        v4u q;
#define S3_RD(d, i) "ds_read_b128 " d ", %[rl] offset:" #i "*1024\n\t"
#define S3_ACC(lo, hi, a, b) A_PKADD(lo, lo, a) A_PKADD(hi, hi, b)
        asm volatile(S3_RD("v[96:99]", 0) S3_RD("v[104:107]", 1) S3_RD("v[108:111]", 2) S3_RD("v[112:115]", 3)
            S3_RD("v[116:119]", 4) S3_RD("v[120:123]", 5) S3_RD("v[124:127]", 6) S3_RD("v[88:91]", 7)
            "s_waitcnt lgkmcnt(7)\n\t" S3_ACC("v[96:97]", "v[98:99]", "%[r0]", "%[r1]")
            "s_waitcnt lgkmcnt(5)\n\t" S3_ACC("v[104:105]", "v[106:107]", "v[108:109]", "v[110:111]")
            "s_waitcnt lgkmcnt(3)\n\t" S3_ACC("v[112:113]", "v[114:115]", "v[116:117]", "v[118:119]") S3_ACC("v[96:97]", "v[98:99]", "v[104:105]", "v[106:107]")
            "s_waitcnt lgkmcnt(1)\n\t" S3_ACC("v[120:121]", "v[122:123]", "v[124:125]", "v[126:127]") S3_ACC("v[96:97]", "v[98:99]", "v[112:113]", "v[114:115]")
            "s_waitcnt lgkmcnt(0)\n\t" S3_ACC("v[120:121]", "v[122:123]", "v[88:89]", "v[90:91]") S3_ACC("v[96:97]", "v[98:99]", "v[120:121]", "v[122:123]")
            "v_cvt_pk_bf16_f32 v96, v96, v97\n\tv_cvt_pk_bf16_f32 v97, v98, v99\n\tv_min_u32 v96, -2, v96\n\tv_min_u32 v97, -2, v97\n\ts_nop 0\n\t"
            "v_mov_b32_dpp v100, v96 quad_perm:[0,0,2,2] row_mask:0xf bank_mask:0xf\n\tv_mov_b32_dpp v101, v97 quad_perm:[0,0,2,2] row_mask:0xf bank_mask:0xf\n\t"
            "v_mov_b32_dpp v102, v96 quad_perm:[1,1,3,3] row_mask:0xf bank_mask:0xf\n\tv_mov_b32_dpp v103, v97 quad_perm:[1,1,3,3] row_mask:0xf bank_mask:0xf"
            : "=&{v[100:103]}"(q) : [rl] "v"(rl), [r0] "v"(res0), [r1] "v"(res1) : Z28_CLOBBER, "v88", "v89", "v90", "v91", "memory");
        st16_sc1(q, xout, rs_ws);
      }
    } else if (role == 4) {
      STAGE_VARS();
      const int j4 = (g - 4) * 27 + r;
      const u32 wf0 = WF0() ^ ((w & 1) * 32);
      const u32 wbase = L_WR + ((w >> 1) & 1) * 40960 + (w >> 2) * 20480;
      v4u wf[10];
      if (it == 0) {
        lds_barrier();
#pragma unroll
        for (int i = NE; i < 10; ++i) ROLE_DMA(i);
        WAITVM(0);
        lds_barrier();
#pragma unroll
        for (int s = 0; s < 10; ++s) wf[s] = wfrag(L + wbase + s * 2048 + wf0);
#pragma unroll
        for (int i = 0; i < 10; ++i) asm volatile("" : "+a"(wf[i]));
      } else {
        idle<SLP_S4>();
#pragma unroll
        for (int s = 0; s < 10; ++s) wf[s] = wfrag(L + wbase + s * 2048 + wf0);
#pragma unroll
        for (int i = 0; i < 10; ++i) asm volatile("" : "+a"(wf[i]));
      }
      u32 rl = L_RR + ((lane >> 3) & 1) * 512 + g4 * 64 + (lane & 7) * 8, hout = (lane & 3) < 2 ? S_HB0 + pbase + (lane & 3) * 13824 + j4 * 256 + (lane >> 2) * 16 : OOB;
      u32 xaddr = (c16 < 4 ? L_XN + c16 * XN_STRIDE : L_ONE) + ((w >> 2) * 320 + 8 * g4) * 2;
      u32 rw = L_RR + (w >> 2) * 2048 + ((w >> 1) & 1) * 1024 + (w & 1) * 512 + c16 * 64 + g4 * 16;
      u32 gw = L_GR + (w < 2 ? w : w - 4) * 128 + ((g4 & 1) * 4 + (c16 & 3)) * 16;
      u32 g2r = L_GR + g4 * 20, g1r = L_GR + 64 + g4 * 16, csr = L_RR + ((lane >> 3) & 1) * 512 + 256 + (lane & 7) * 8;
      LAUNDER_V(rl); LAUNDER_V(hout); LAUNDER_V(xaddr); LAUNDER_V(rw); LAUNDER_V(gw); LAUNDER_V(g2r); LAUNDER_V(g1r); LAUNDER_V(csr);
      {
        const int t0 = w < 4 ? 3 * w : 2 * w + 4;
        u32 o1 = S_X1P0 + pbase + (g - 4) * 40960 + g4 * 5120 + (16 * t0 + c16) * 16;
        u32 o2 = w < 4 ? o1 + 512 : OOB;
        v4u xsel = sum_selector(c16);
        u32 xw[3];
#pragma unroll
        for (int T = 0; T < 3; ++T) { const u32 P = 16 * (t0 + T) + c16; xw[T] = L_XN + ((P >> 3) & 3) * XN_STRIDE + (P >> 5) * 128 + (P & 7) * 16 + (g4 & 1) * 8; }
        LAUNDER_V(o1); LAUNDER_V(o2); LAUNDER_V(xw[0]); LAUNDER_V(xw[1]); LAUNDER_V(xw[2]);
        u32 cnt = SPIN_MAX;
#define M3(d, a, b, c) "v_max3_u32 " d ", " a ", " b ", " c "\n\t"
#define X1MF(d, b, c) "v_mfma_f32_16x16x32_bf16 " d ", v[92:95], " b ", " c "\n\t"
        asm volatile( "s_movk_i32 s66, 0x5000\n" A_POLL
            "buffer_load_dwordx4 v[96:99], %[o1], %[rs], 0 offen sc1\n\tbuffer_load_dwordx4 v[100:103], %[o1], %[rs], s66 offen sc1\n\t"
            "buffer_load_dwordx4 v[104:107], %[o1], %[rs], 0 offen offset:256 sc1\n\tbuffer_load_dwordx4 v[108:111], %[o1], %[rs], s66 offen offset:256 sc1\n\t"
            "buffer_load_dwordx4 v[112:115], %[o2], %[rs], 0 offen sc1\n\tbuffer_load_dwordx4 v[116:119], %[o2], %[rs], s66 offen sc1\n\ts_waitcnt vmcnt(0)\n\t"
            M3("v88", "v96", "v97", "v98") M3("v89", "v99", "v100", "v101") M3("v90", "v102", "v103", "v104") M3("v91", "v105", "v106", "v107")
            M3("v120", "v108", "v109", "v110") M3("v121", "v111", "v112", "v113") M3("v122", "v114", "v115", "v116") M3("v123", "v117", "v118", "v119")
            M3("v88", "v88", "v89", "v90") M3("v91", "v91", "v120", "v121") M3("v88", "v88", "v122", "v123") "v_max_u32 v88, v88, v91\n\t"
            A_DONE("v88")
            X1MF("v[120:123]", "v[96:99]", "0") X1MF("v[124:127]", "v[104:107]", "0") X1MF("v[88:91]", "v[112:115]", "0")
            X1MF("v[120:123]", "v[100:103]", "v[120:123]") X1MF("v[124:127]", "v[108:111]", "v[124:127]") X1MF("v[88:91]", "v[116:119]", "v[88:91]")
            "s_mov_b32 exec_hi, 0\n\ts_nop 4\n\t"
            "v_cvt_pk_bf16_f32 v96, v120, v121\n\tv_cvt_pk_bf16_f32 v97, v122, v123\n\tds_write_b64 %[x0], v[96:97]\n\t"
            "v_cvt_pk_bf16_f32 v98, v124, v125\n\tv_cvt_pk_bf16_f32 v99, v126, v127\n\tds_write_b64 %[x1], v[98:99]\n\t"
            "s_cmp_gt_u32 %[w], 3\n\ts_cbranch_scc1 .Ls%=\n\t"
            "v_cvt_pk_bf16_f32 v100, v88, v89\n\tv_cvt_pk_bf16_f32 v101, v90, v91\n\tds_write_b64 %[x2], v[100:101]\n"
            ".Ls%=:\n\ts_mov_b64 exec, -1\n\t"
            : [cnt] "+s"(cnt), "+{v[92:95]}"(xsel)
            : [o1] "v"(o1), [o2] "v"(o2), [x0] "v"(xw[0]), [x1] "v"(xw[1]), [x2] "v"(xw[2]), [rs] "s"(rs_ws), [w] "s"(w)
            : Z32_CLOBBER, "v88", "v89", "v90", "v91", "s66", "vcc", "scc", "memory");
      }
#define XRD(d, off) "ds_read_b128 " d ", %[xa] offset:" off "\n\t"
#define LGK(n) "s_waitcnt lgkmcnt(" n ")\n\t"
#define S4_MF(wi, b) "v_mfma_f32_16x16x32_bf16 %[acc], %[" wi "], " b ", %[acc]\n\t"
#define S4_MF0(wi, b) "v_mfma_f32_16x16x32_bf16 %[acc], %[" wi "], " b ", 0\n\t"
#define GRAM0(b) "v_mfma_f32_16x16x32_bf16 v[96:99], " b ", " b ", 0\n\t"
#define GRAM(b) "v_mfma_f32_16x16x32_bf16 v[96:99], " b ", " b ", v[96:99]\n\t"
#define S4_HEAD XRD("v[104:107]", "0") XRD("v[108:111]", "64") XRD("v[112:115]", "128") XRD("v[116:119]", "192") XRD("v[120:123]", "256") XRD("v[124:127]", "320")
#define S4_W [w0] "a"(wf[0]), [w1] "a"(wf[1]), [w2] "a"(wf[2]), [w3] "a"(wf[3]), [w4] "a"(wf[4]), [w5] "a"(wf[5]), [w6] "a"(wf[6]), [w7] "a"(wf[7]), \
             [w8] "a"(wf[8]), [w9] "a"(wf[9]), [xa] "v"(xaddr), [rw] "v"(rw), [gw] "v"(gw)
#define S4_BLOCK(G0, G1, G2, G3, G4, G5, G6, G7, G8, G9, GW) asm volatile(A_BAR "\n\t" S4_HEAD \
            LGK("5") S4_MF0("w0", "v[104:107]") G0 XRD("v[104:107]", "384") \
            LGK("5") S4_MF("w1", "v[108:111]") G1 XRD("v[108:111]", "448") \
            LGK("5") S4_MF("w2", "v[112:115]") G2 XRD("v[112:115]", "512") \
            LGK("5") S4_MF("w3", "v[116:119]") G3 XRD("v[116:119]", "576") \
            LGK("5") S4_MF("w4", "v[120:123]") G4 LGK("4") S4_MF("w5", "v[124:127]") G5 LGK("3") S4_MF("w6", "v[104:107]") G6 \
            LGK("2") S4_MF("w7", "v[108:111]") G7 LGK("1") S4_MF("w8", "v[112:115]") G8 LGK("0") S4_MF("w9", "v[116:119]") G9 \
            "s_nop 5\n\t" A_EXEC_C5 "ds_write_b128 %[rw], %[acc]\n\t" GW A_EXEC_ALL A_BAR \
            : [acc] "=&v"(acc) : S4_W : Z32_CLOBBER, "memory")
#define S4_GW "s_mov_b32 exec_lo, 0x000f000f\n\ts_mov_b32 exec_hi, 0\n\tds_write_b128 %[gw], v[96:99]\n\t"
      f4 acc;
      if (w == 0 || w == 6) {
        S4_BLOCK(GRAM0("v[104:107]"), GRAM("v[108:111]"), GRAM("v[112:115]"), GRAM("v[116:119]"), GRAM("v[120:123]"), "", "", "", "", "", S4_GW);
      } else if (w == 1 || w == 7) {
        S4_BLOCK("", "", "", "", "", GRAM0("v[124:127]"), GRAM("v[104:107]"), GRAM("v[108:111]"), GRAM("v[112:115]"), GRAM("v[116:119]") "s_nop 0\n\t", S4_GW);
      } else {
        S4_BLOCK("", "", "", "", "", "", "", "", "", "", "");
      }
      if (w == 0) {
        v4u q;
        asm volatile(
            "ds_read2_b32 v[120:121], %[g1r] offset0:0 offset1:32\n\tds_read2_b32 v[122:123], %[g1r] offset0:64 offset1:96\n\t"
            "ds_read2_b32 v[124:125], %[g2r] offset0:0 offset1:32\n\tds_read2_b32 v[126:127], %[g2r] offset0:64 offset1:96\n\t"
            "ds_read2st64_b64 v[96:99], %[rl] offset0:0 offset1:4\n\tds_read2st64_b64 v[104:107], %[rl] offset0:2 offset1:6\n\t"
            "ds_read2st64_b64 v[112:115], %[csr] offset0:0 offset1:4\n\tds_read2st64_b64 v[116:119], %[csr] offset0:2 offset1:6\n\t"
            "s_waitcnt lgkmcnt(6)\n\t" A_PKADD("v[120:121]", "v[120:121]", "v[122:123]")
            "s_waitcnt lgkmcnt(4)\n\t" A_PKADD("v[124:125]", "v[124:125]", "v[126:127]")
            "v_add_f32 v108, v120, v121\n\tv_add_f32 v109, v124, v125\n\tv_mul_f32 v108, 0x3acccccd, v108\n\tv_mul_f32 v109, 0x3acccccd, v109\n\t"
            "s_waitcnt lgkmcnt(3)\n\t" A_PKADD("v[96:97]", "v[96:97]", "v[98:99]")
            "v_fma_f32 v109, -v108, v108, v109\n\tv_max_f32 v109, 0, v109\n\t"
            "s_waitcnt lgkmcnt(2)\n\t" A_PKADD("v[104:105]", "v[104:105]", "v[106:107]")
            "v_add_f32 v109, 0x3727c5ac, v109\n\tv_rsq_f32 v109, v109\n\t"
            "s_waitcnt lgkmcnt(1)\n\t" A_PKADD("v[112:113]", "v[112:113]", "v[114:115]")
            "s_waitcnt lgkmcnt(0)\n\t" A_PKADD("v[114:115]", "v[116:117]", "v[118:119]")
            "v_pk_fma_f32 v[96:97], v[112:113], v[108:109], v[96:97] op_sel_hi:[1,0,1] neg_lo:[0,1,0] neg_hi:[0,1,0]\n\t"
            "v_pk_fma_f32 v[104:105], v[114:115], v[108:109], v[104:105] op_sel_hi:[1,0,1] neg_lo:[0,1,0] neg_hi:[0,1,0]\n\t"
            "v_pk_mul_f32 v[96:97], v[96:97], v[108:109] op_sel:[0,1] op_sel_hi:[1,1]\n\t"
            "v_pk_mul_f32 v[104:105], v[104:105], v[108:109] op_sel:[0,1] op_sel_hi:[1,1]\n\t"
            "v_cmp_lt_f32 vcc, 0, v96\n\tv_mul_f32 v98, 0, v96\n\tv_mul_f32 v99, 0, v97\n\tv_cndmask_b32 v96, v98, v96, vcc\n\t"
            "v_cmp_lt_f32 vcc, 0, v97\n\ts_nop 1\n\tv_cndmask_b32 v97, v99, v97, vcc\n\t"
            "v_pk_mul_f32 v[96:97], v[96:97], v[104:105]\n\t"
            A_PACK_QUAD
            : "=&{v[100:103]}"(q) : [rl] "v"(rl), [g1r] "v"(g1r), [g2r] "v"(g2r), [csr] "v"(csr) : Z28_CLOBBER, "vcc", "memory");
        st16_sc1(q, hout, rs_ws);
      }
    } else if (role == 5) {
      STAGE_VARS();
      const int s0 = w < 6 ? 7 * w : 42 + 6 * (w - 6), n5 = (g - 6) * 20 + r;
      if (it == 0) {
        if (NE5 < 7) {
          lds_barrier();
#pragma unroll
          for (int i = NE5; i < 6; ++i) ROLE_DMA(i);
          if (w < 6) ROLE_DMA(6);
        }
        WAITVM(0);
        idle<SLP_S5_0>();
      } else {
        idle<SLP_S5>();
      }
      v4u wf[7];
      {
        const u32 wf0 = L_WR + ((8 * g4 + (c16 >> 2)) ^ ((g4 & 1) << 2)) * 32 + (c16 & 3) * 8;
#pragma unroll
        for (int s = 0; s < 6; ++s) wf[s] = wfrag2(L + (s0 + s) * 1024 + wf0, L + (s0 + s) * 1024 + (wf0 ^ 128));
        const int s6 = w < 6 ? s0 + 6 : s0;
        wf[6] = wfrag2(L + s6 * 1024 + wf0, L + s6 * 1024 + (wf0 ^ 128));
      }
#pragma unroll
      for (int i = 0; i < 7; ++i) asm volatile("" : "+a"(wf[i]));
      const int slot = c16 >> 2, l5 = lane & 31;
      const u32 hoff = S_HB0 + pbase + (g - 6) * 13824 + (c16 & 3) * 64 + g4 * 16;
      u32 oA = hoff + (s0 + slot) * 256;
      u32 oB = (slot < (w < 6 ? 3 : 2)) ? hoff + (s0 + 4 + slot) * 256 : OOB;
      u32 rl = L_RR + (l5 >> 3) * 64 + (l5 & 7) * 8, rw = L_RR + w * 256 + c16 * 64 + g4 * 16;
      u32 xout = it == 0 ? S_X2 + ((lane & 3) + 4 * (lane >> 5)) * 5120 + n5 * 128 + (l5 >> 2) * 16
                         : ((lane & 35) ? OOB : (u32)((l5 >> 3) * 1280 + n5 * 32 + ((l5 >> 2) & 1) * 16));
      f2 res = {0.f, 0.f};
      if (w == 0) {
        const u32 rxo = S_X1P0 + pbase + (n5 >> 2) * 512 + (l5 >> 3) * 128 + (n5 & 3) * 32 + (l5 & 7) * 4;
        u32 rx[8];
        _Pragma("nounroll") for (u32 spin = 0; spin < SPIN_MAX; ++spin) {
#pragma unroll
          for (int e = 0; e < 8; ++e) asm volatile("buffer_load_dword %0, %1, %2, 0 offen sc1" : "=v"(rx[e]) : "v"(rxo + e * 5120), "s"(rs_ws) : "memory");
          asm volatile("s_waitcnt vmcnt(0)" : "+v"(rx[0]), "+v"(rx[1]), "+v"(rx[2]), "+v"(rx[3]), "+v"(rx[4]), "+v"(rx[5]), "+v"(rx[6]), "+v"(rx[7])::"memory");
          if (all_valid(umax3(umax3(umax3(rx[0], rx[1], rx[2]), rx[3], rx[4]), umax3(rx[5], rx[6], rx[7]), 0u))) break;
          __builtin_amdgcn_s_sleep(4);
        }
#pragma unroll
        for (int e = 0; e < 8; ++e) res += f2{bflo(rx[e]), bfhi(rx[e])};
      }
      LAUNDER_V(oA); LAUNDER_V(oB); LAUNDER_V(rl); LAUNDER_V(rw); LAUNDER_V(xout); LAUNDER_V(res);
      {
        u32 cnt = SPIN_MAX;
        asm volatile( A_POLL
            "buffer_load_dwordx4 v[96:99], %[oa], %[rs], 0 offen sc1\n\tbuffer_load_dwordx4 v[100:103], %[ob], %[rs], 0 offen sc1\n\ts_waitcnt vmcnt(0)\n\t"
            "v_max3_u32 v112, v96, v97, v98\n\tv_max3_u32 v113, v99, v100, v101\n\tv_max3_u32 v112, v112, v102, v103\n\tv_max_u32 v112, v112, v113\n\t"
            A_DONE("v112")
            MF("v[104:107]", "%[w0]", "v[96:99]", "0") MVT("v96", "v97", "v98", "v99", "4") MVU("v96", "v97", "v98", "v99", "8")
            MF("v[104:107]", "%[w4]", "v[100:103]", "v[104:107]") MF("v[104:107]", "%[w1]", RT, "v[104:107]") MVT("v96", "v97", "v98", "v99", "12")
            MF("v[104:107]", "%[w2]", RU, "v[104:107]") MVU("v100", "v101", "v102", "v103", "4")
            MF("v[104:107]", "%[w3]", RT, "v[104:107]") MVT("v100", "v101", "v102", "v103", "8")
            MF("v[104:107]", "%[w5]", RU, "v[104:107]") "s_nop 0\n\t" MF("v[104:107]", "%[w6]", RT, "v[104:107]")
            "s_nop 5\n\t" A_EXEC_C4 "ds_write_b128 %[rw], v[104:107]\n\t" A_EXEC_ALL A_BAR
            : [cnt] "+s"(cnt)
            : [oa] "v"(oA), [ob] "v"(oB), [rw] "v"(rw), [rs] "s"(rs_ws),
              [w0] "a"(wf[0]), [w1] "a"(wf[1]), [w2] "a"(wf[2]), [w3] "a"(wf[3]), [w4] "a"(wf[4]), [w5] "a"(wf[5]), [w6] "a"(wf[6])
            : Z32_CLOBBER, "vcc", "scc", "memory");
      }
      if (w == 0) {
        v4u q;
        asm volatile("ds_read2_b64 v[96:99], %[rl] offset0:0 offset1:32\n\tds_read2_b64 v[104:107], %[rl] offset0:64 offset1:96\n\t"
            "ds_read2_b64 v[108:111], %[rl] offset0:128 offset1:160\n\tds_read2_b64 v[112:115], %[rl] offset0:192 offset1:224\n\t"
            "s_waitcnt lgkmcnt(3)\n\t" A_PKADD("v[96:97]", "v[96:97]", "v[98:99]") A_PKADD("v[96:97]", "v[96:97]", "%[res]")
            "s_waitcnt lgkmcnt(2)\n\t" A_PKADD("v[104:105]", "v[104:105]", "v[106:107]")
            "s_waitcnt lgkmcnt(1)\n\t" A_PKADD("v[108:109]", "v[108:109]", "v[110:111]") A_PKADD("v[96:97]", "v[96:97]", "v[104:105]")
            "s_waitcnt lgkmcnt(0)\n\t" A_PKADD("v[112:113]", "v[112:113]", "v[114:115]") A_PKADD("v[96:97]", "v[96:97]", "v[108:109]")
            A_PKADD("v[96:97]", "v[96:97]", "v[112:113]") A_PACK_QUAD
            : "=&{v[100:103]}"(q) : [rl] "v"(rl), [res] "v"(res) : Z28_CLOBBER, "memory");
        if (it == 0) {
          st16_sc1(q, xout, rs_ws);
        } else {
          const rsrc_t rs_o = mk_rsrc(outp, 4 * DM * 2);
          asm volatile("s_nop 4\n\tbuffer_store_dwordx4 %0, %1, %2, 0 offen\n\ts_nop 1" ::"v"(q), "v"(xout), "s"(rs_o) : "memory");
          if (n5 == 0) st8_sc1(v2u{tag_lo, tag_hi}, lane == 0 ? WS_G : OOB, rs_ws);
        }
      }
    }
  }
}

}

extern "C" void kernel_launch(void* const* d_in, const int* in_sizes, int n_in, void* d_out, int out_size, void* d_ws,
                              size_t ws_size, hipStream_t stream) {
  Args a;
  a.x = (const u16*)d_in[0];
  a.k = (const u16*)d_in[1];
  a.v = (const u16*)d_in[2];
  a.wq = (const u16*)d_in[3];
  a.wo = (const u16*)d_in[4];
  a.wg = (const u16*)d_in[6];
  a.wu = (const u16*)d_in[7];
  a.wd = (const u16*)d_in[8];
  a.out = (u16*)d_out;
  a.ws = (unsigned char*)d_ws;
  (void)hipFuncSetAttribute((const void*)fused_decode, hipFuncAttributeMaxDynamicSharedMemorySize, (int)L_END);
  hipLaunchKernelGGL(fused_decode, dim3(NWG), dim3(NT), L_END, stream, a);
}
```
